```python
import torch

def _softmax(x, axis=-1):
    return torch.softmax(x, dim=axis)
def _gelu(x):
    return torch.nn.functional.gelu(x, approximate="tanh")
def _silu(x):
    return torch.nn.functional.silu(x)
def _sigmoid(x):
    return torch.sigmoid(x)
def _layernorm(x, axis=-1, eps=1e-5):
    m = x.mean(dim=axis, keepdim=True)
    v = x.var(dim=axis, keepdim=True, unbiased=False)
    return (x - m) / torch.sqrt(v + eps)
def _scatter_add(tgt, idx, src, axis=0):
    return tgt.clone().index_add_(axis, idx, src)
def _segment_sum(src, seg, n):
    out = torch.zeros((n,) + src.shape[1:], dtype=src.dtype)
    return out.index_add_(0, seg, src)
def _pad(x, pw):
    flat = tuple(v for pair in reversed(pw) for v in pair)
    return torch.nn.functional.pad(x, flat)

kernel_name = 'concat_heads__linear_kernel_hedgehog__linear_kernel_hedgehog__prompt'

def setup_inputs():

    _w = (lambda a: (lambda t: t.to(torch.bfloat16) if t.is_floating_point() else t)(torch.as_tensor(a)))
    def _A_setup():
        B, S, D, H = 1, 2048, 512, 32
        Dh = D // H
        return {
            "x": _w((lambda *s: torch.randn(*s, dtype=torch.bfloat16) * 0.4)(B, S, D)),
            "wqkv": _w((lambda *s: torch.randn(*s, dtype=torch.bfloat16) * 0.4)(D, 3 * D)),
            "w_phi": _w((lambda *s: torch.randn(*s, dtype=torch.bfloat16) * 0.4)(H, Dh, Dh) * (Dh ** -0.5)),
            "wo": _w((lambda *s: torch.randn(*s, dtype=torch.bfloat16) * 0.05)(D, D)),
        }
    _A_in = _A_setup()

    def _B_setup():
        B, S, D, H = 1, 2048, 512, 32
        Dh = D // H
        return {
            "x": _w((lambda *s: torch.randn(*s, dtype=torch.bfloat16) * 0.4)(B, S, D)),
            "wqkv": _w((lambda *s: torch.randn(*s, dtype=torch.bfloat16) * 0.4)(D, 3 * D)),
            "w_phi": _w((lambda *s: torch.randn(*s, dtype=torch.bfloat16) * 0.4)(H, Dh, Dh) * (Dh ** -0.5)),
            "wo": _w((lambda *s: torch.randn(*s, dtype=torch.bfloat16) * 0.05)(D, D)),
        }
    _B_in = _B_setup()

    return {
        "A_x": _A_in["x"],
        "A_wqkv": _A_in["wqkv"],
        "A_w_phi": _A_in["w_phi"],
        "A_wo": _A_in["wo"],
        "B_x": _B_in["x"],
        "B_wqkv": _B_in["wqkv"],
        "B_w_phi": _B_in["w_phi"],
        "B_wo": _B_in["wo"],
    }

def reference(A_x, A_wqkv, A_w_phi, A_wo, B_x, B_wqkv, B_w_phi, B_wo):
    def _A_ref(x, wqkv, w_phi, wo):
        B, S, D = x.shape
        H = 32; Dh = D // H
        qkv = (lambda x, *d: x.permute(*d))((x @ wqkv).reshape(B, S, 3, H, Dh), 2, 0, 3, 1, 4)
        q, k, v = qkv[0], qkv[1], qkv[2]
        pq = torch.einsum("bhsd,hdf->bhsf", q, w_phi)
        pk = torch.einsum("bhsd,hdf->bhsf", k, w_phi)
        phi_q = torch.concatenate([_softmax(pq, axis=-1), _softmax(-pq, axis=-1)], axis=-1)
        phi_k = torch.concatenate([_softmax(pk, axis=-1), _softmax(-pk, axis=-1)], axis=-1)
        kv = torch.einsum("bhsf,bhsd->bhfd", phi_k, v)
        z  = torch.einsum("bhsf->bhf", phi_k)
        num = torch.einsum("bhqf,bhfd->bhqd", phi_q, kv)
        den = torch.einsum("bhqf,bhf->bhq", phi_q, z)[..., None] + 1e-6
        o = num / den
        return (lambda x, *d: x.permute(*d))(o, 0, 2, 1, 3).reshape(B, S, D) @ wo

    def _B_ref(x, wqkv, w_phi, wo):
        B, S, D = x.shape
        H = 32; Dh = D // H
        qkv = (lambda x, *d: x.permute(*d))((x @ wqkv).reshape(B, S, 3, H, Dh), 2, 0, 3, 1, 4)
        q, k, v = qkv[0], qkv[1], qkv[2]
        pq = torch.einsum("bhsd,hdf->bhsf", q, w_phi)
        pk = torch.einsum("bhsd,hdf->bhsf", k, w_phi)
        phi_q = torch.concatenate([_softmax(pq, axis=-1), _softmax(-pq, axis=-1)], axis=-1)
        phi_k = torch.concatenate([_softmax(pk, axis=-1), _softmax(-pk, axis=-1)], axis=-1)
        kv = torch.einsum("bhsf,bhsd->bhfd", phi_k, v)
        z  = torch.einsum("bhsf->bhf", phi_k)
        num = torch.einsum("bhqf,bhfd->bhqd", phi_q, kv)
        den = torch.einsum("bhqf,bhf->bhq", phi_q, z)[..., None] + 1e-6
        o = num / den
        return (lambda x, *d: x.permute(*d))(o, 0, 2, 1, 3).reshape(B, S, D) @ wo

    a = _A_ref(A_x, A_wqkv, A_w_phi, A_wo)
    b = _B_ref(B_x, B_wqkv, B_w_phi, B_wo)
    return torch.concatenate([a, b], axis=-1)
```

```cpp
#include <hip/hip_runtime.h>
#include <hip/hip_bf16.h>
typedef unsigned short u16;
__device__ __forceinline__ float bf2f(u16 x){ return __uint_as_float(((unsigned)x)<<16); }
__device__ __forceinline__ u16 f2bf(float x){ unsigned u=__float_as_uint(x); u += 0x7fffu + ((u>>16)&1u); return (u16)(u>>16); }
constexpr int S=2048, D=512, H=32, DH=16;

__global__ void k_qkv(const u16* xA,const u16* wA,const u16* xB,const u16* wB,float* qkv){
  int br=blockIdx.z; const u16* x= br? xB:xA; const u16* w= br? wB:wA;
  int n=blockIdx.x*256+threadIdx.x; int s=blockIdx.y;
  float acc=0; for(int k=0;k<D;k++) acc+=bf2f(x[s*D+k])*bf2f(w[k*1536+n]);
  qkv[((size_t)br*S+s)*1536+n]=bf2f(f2bf(acc));
}
__global__ void k_phi(const float* qkv,const u16* pA,const u16* pB,float* phi){
  int br=blockIdx.z; int qk=blockIdx.y; const u16* wp= br? pB:pA;
  int idx=blockIdx.x*256+threadIdx.x; int s=idx/H, h=idx%H;
  const float* q=qkv+((size_t)br*S+s)*1536+qk*512+h*16;
  float p[16]; float mx=-1e30f, mn=1e30f;
  #pragma unroll
  for(int f=0;f<16;f++){ float a=0;
    #pragma unroll
    for(int d=0;d<16;d++) a+=q[d]*bf2f(wp[h*256+d*16+f]); p[f]=a; mx=fmaxf(mx,a); mn=fminf(mn,a);}
  float s1=0,s2=0; float e1[16],e2[16];
  #pragma unroll
  for(int f=0;f<16;f++){ e1[f]=__expf(p[f]-mx); e2[f]=__expf(mn-p[f]); s1+=e1[f]; s2+=e2[f]; }
  float* o=phi+((((size_t)br*2+qk)*S+s)*H+h)*32;
  #pragma unroll
  for(int f=0;f<16;f++){ o[f]=bf2f(f2bf(e1[f]/s1)); o[16+f]=bf2f(f2bf(e2[f]/s2)); }
}
__global__ void k_kv(const float* qkv,const float* phi,float* kv){
  int br=blockIdx.y, h=blockIdx.x; int t=threadIdx.x; if(t>=32*17) return; int f=t/17,d=t%17;
  float acc=0;
  for(int s=0;s<S;s++){ float pk=phi[((((size_t)br*2+1)*S+s)*H+h)*32+f]; float v= d<16? qkv[((size_t)br*S+s)*1536+1024+h*16+d]:1.f; acc+=pk*v; }
  kv[((size_t)br*H+h)*544+t]=bf2f(f2bf(acc));
}
__global__ void k_o(const float* phi,const float* kv,float* o){
  int br=blockIdx.z; int idx=blockIdx.x*256+threadIdx.x; int s=idx/512, c=idx%512, h=c/16,d=c%16;
  const float* pq=phi+((((size_t)br*2+0)*S+s)*H+h)*32; const float* K=kv+((size_t)br*H+h)*544;
  float num=0,den=0; for(int f=0;f<32;f++){ num+=pq[f]*K[f*17+d]; den+=pq[f]*K[f*17+16]; }
  o[((size_t)br*S+s)*512+c]=bf2f(f2bf(num/(den+1e-6f)));
}
__global__ void k_out(const float* o,const u16* wA,const u16* wB,u16* out){
  int br=blockIdx.z; const u16* w= br? wB:wA; int n=blockIdx.x*256+threadIdx.x; int s=blockIdx.y;
  float acc=0; for(int k=0;k<D;k++) acc+=o[((size_t)br*S+s)*512+k]*bf2f(w[k*512+n]);
  out[(size_t)s*1024+br*512+n]=f2bf(acc);
}
extern "C" void kernel_launch(void* const* d_in, const int* in_sizes, int n_in, void* d_out, int out_size, void* d_ws, size_t ws_size, hipStream_t stream) {
  const u16* xA=(const u16*)d_in[0]; const u16* wA=(const u16*)d_in[1]; const u16* pA=(const u16*)d_in[2]; const u16* oA=(const u16*)d_in[3];
  const u16* xB=(const u16*)d_in[4]; const u16* wB=(const u16*)d_in[5]; const u16* pB=(const u16*)d_in[6]; const u16* oB=(const u16*)d_in[7];
  float* qkv=(float*)d_ws; float* phi=qkv+(size_t)2*S*1536; float* kv=phi+(size_t)2*2*S*H*32; float* o=kv+2*H*544;
  k_qkv<<<dim3(6,S,2),256,0,stream>>>(xA,wA,xB,wB,qkv);
  k_phi<<<dim3(S*H/256,2,2),256,0,stream>>>(qkv,pA,pB,phi);
  k_kv<<<dim3(H,2),576,0,stream>>>(qkv,phi,kv);
  k_o<<<dim3(S*512/256,1,2),256,0,stream>>>(phi,kv,o);
  k_out<<<dim3(2,S,2),256,0,stream>>>(o,oA,oB,(u16*)d_out);
}
```

```cpp
#include <hip/hip_runtime.h>
#include <hip/hip_bf16.h>
#pragma clang diagnostic ignored "-Wint-to-pointer-cast"

typedef unsigned short u16;
typedef unsigned long long u64;
typedef __bf16 bf16x8 __attribute__((ext_vector_type(8)));
typedef __bf16 bf16x4 __attribute__((ext_vector_type(4)));
typedef __bf16 bf16x2 __attribute__((ext_vector_type(2)));
typedef float f32x16 __attribute__((ext_vector_type(16)));
typedef float f32x4 __attribute__((ext_vector_type(4)));
typedef float f32x2 __attribute__((ext_vector_type(2)));
typedef unsigned u32x4 __attribute__((ext_vector_type(4)));
typedef unsigned u32x2 __attribute__((ext_vector_type(2)));
#define LAS __attribute__((address_space(3)))
#define GAS __attribute__((address_space(1)))
#define DI __device__ __forceinline__
typedef __amdgpu_buffer_rsrc_t rsrc_t;

#define STAMPS 0

constexpr unsigned WS_EPOCH = 0;
constexpr unsigned WS_FLAG  = 4096;
constexpr unsigned WS_KVP   = 65536;
constexpr unsigned KVP_STR  = 4608;
constexpr unsigned WS_OF    = 2u << 20;
constexpr unsigned WS_BYTES = 6u << 20;
constexpr unsigned WS_STAMP = 8u << 20;
constexpr unsigned SLOT  = 32768;
constexpr unsigned L_V    = 131072;
constexpr unsigned L_PHIW = 147456;
constexpr unsigned L_ZERO = L_PHIW + 2048;
constexpr unsigned L_ONES = L_ZERO + 16;
constexpr unsigned L_KVF  = L_PHIW + 2560;
constexpr unsigned L_BYTES = 163840;
constexpr unsigned SPIN_CAP = 1u << 16;

#define WAITV(n) asm volatile("s_waitcnt vmcnt(" #n ")" ::: "memory")
#define WAITL0() asm volatile("s_waitcnt lgkmcnt(0)" ::: "memory")
#define BAR() do { WAITL0(); __builtin_amdgcn_s_barrier(); asm volatile("" ::: "memory"); } while (0)

DI void dma16(unsigned lds, unsigned voff, rsrc_t rs, unsigned soff) {
  asm volatile("s_mov_b32 m0, %0\n\ts_nop 0\n\tbuffer_load_dwordx4 %1, %2, %3 offen lds" :: "s"(lds), "v"(voff), "s"(rs), "s"(soff) : "memory");
}
DI void dma4(unsigned lds, unsigned voff, rsrc_t rs, unsigned soff) {
  asm volatile("s_mov_b32 m0, %0\n\ts_nop 0\n\tbuffer_load_dword %1, %2, %3 offen lds" :: "s"(lds), "v"(voff), "s"(rs), "s"(soff) : "memory");
}
DI void st16_sc1(u32x4 v, unsigned voff, rsrc_t rs, unsigned soff) {
  asm volatile("buffer_store_dwordx4 %0, %1, %2, %3 offen sc1\n\ts_nop 1" :: "v"(v), "v"(voff), "s"(rs), "s"(soff) : "memory");
}
DI void ld16_sc1(u32x4& v, unsigned voff, rsrc_t rs, unsigned soff) {
  asm volatile("buffer_load_dwordx4 %0, %1, %2, %3 offen sc1" : "=v"(v) : "v"(voff), "s"(rs), "s"(soff) : "memory");
}
DI bf16x8 lds_b128(unsigned a) { return *(const LAS bf16x8*)a; }
DI bf16x8 lds_tr(unsigned a0, unsigned a1) {
  bf16x4 lo = __builtin_amdgcn_ds_read_tr16_b64_v4bf16((LAS bf16x4*)a0);
  bf16x4 hi = __builtin_amdgcn_ds_read_tr16_b64_v4bf16((LAS bf16x4*)a1);
  return __builtin_shufflevector(lo, hi, 0, 1, 2, 3, 4, 5, 6, 7);
}
DI f32x16 mfma32(bf16x8 a, bf16x8 b, f32x16 c) { return __builtin_amdgcn_mfma_f32_32x32x16_bf16(a, b, c, 0, 0, 0); }
DI f32x4 mfma16(bf16x8 a, bf16x8 b, f32x4 c) { return __builtin_amdgcn_mfma_f32_16x16x32_bf16(a, b, c, 0, 0, 0); }
template <int O> DI bf16x8 cvt8(const f32x16& c) {
  bf16x8 r;
#pragma unroll
  for (int i = 0; i < 8; ++i) r[i] = (__bf16)c[O + i];
  return r;
}
DI u32x4 nopoison(bf16x8 v) {
  u32x4 u = __builtin_bit_cast(u32x4, v);
#pragma unroll
  for (int i = 0; i < 4; ++i) u[i] = u[i] < 0xFFFFFFFEu ? u[i] : 0xFFFFFFFEu;
  return u;
}
DI unsigned umax4(u32x4 a) { return max(max(a[0], a[1]), max(a[2], a[3])); }

DI void dual_softmax(const f32x16& p, bf16x8 (&f)[4]) {
  float mx = __builtin_fmaxf(p[0], p[1]), mn = __builtin_fminf(p[0], p[1]);
#pragma unroll
  for (int i = 2; i < 16; i += 2) { mx = __builtin_fmaxf(__builtin_fmaxf(mx, p[i]), p[i + 1]); mn = __builtin_fminf(__builtin_fminf(mn, p[i]), p[i + 1]); }
  const float L2E = 1.4426950408889634f;
  const float a = -mx * L2E, b = mn * L2E;
  f32x2 e[8], g[8];
  const f32x2 c2 = {L2E, L2E}, a2 = {a, a}, b2 = {b, b};
#pragma unroll
  for (int i = 0; i < 8; ++i) {
    f32x2 v = {p[2 * i], p[2 * i + 1]};
    f32x2 x = v * c2 + a2, y = b2 - v * c2;
    e[i] = f32x2{__builtin_amdgcn_exp2f(x.x), __builtin_amdgcn_exp2f(x.y)};
    g[i] = f32x2{__builtin_amdgcn_exp2f(y.x), __builtin_amdgcn_exp2f(y.y)};
  }
  f32x2 se = ((e[0] + e[1]) + (e[2] + e[3])) + ((e[4] + e[5]) + (e[6] + e[7]));
  f32x2 sg = ((g[0] + g[1]) + (g[2] + g[3])) + ((g[4] + g[5]) + (g[6] + g[7]));
  const float re = __builtin_amdgcn_rcpf(se.x + se.y), rg = __builtin_amdgcn_rcpf(sg.x + sg.y);
  const f32x2 re2 = {re, re}, rg2 = {rg, rg};
#pragma unroll
  for (int i = 0; i < 8; ++i) {
    f32x2 x = e[i] * re2, y = g[i] * rg2;
    f[i >> 2][2 * (i & 3)] = (__bf16)x.x; f[i >> 2][2 * (i & 3) + 1] = (__bf16)x.y;
    f[2 + (i >> 2)][2 * (i & 3)] = (__bf16)y.x; f[2 + (i >> 2)][2 * (i & 3) + 1] = (__bf16)y.y;
  }
}

#define STAMP(i) do {} while (0)
#define STAMPR(i) do {} while (0)
#define STAMPV(i, x) do {} while (0)

__global__ __launch_bounds__(512, 2) void hedgehog_fused(
    const u16* __restrict__ xA, const u16* __restrict__ wA, const u16* __restrict__ pA, const u16* __restrict__ oA,
    const u16* __restrict__ xB, const u16* __restrict__ wB, const u16* __restrict__ pB, const u16* __restrict__ oB,
    u16* __restrict__ out, char* __restrict__ ws) {
  extern __shared__ __attribute__((aligned(16))) char smem[];
  STAMPR(60); STAMP(0);
  const unsigned L = (unsigned)(size_t)(LAS char*)smem;
  const int b = blockIdx.x, xcd = b & 7, jj = b >> 3;
  const int br = xcd >> 2, rt = (xcd & 3) * 4 + (jj >> 3), hg = jj & 7;
  const int rot = (hg + 2 * (rt & 3)) & 7;
  const int tid = threadIdx.x, lane = tid & 63;
  const int w = __builtin_amdgcn_readfirstlane(tid >> 6);
  const int tg = w & 3, hp = w >> 2;
  const int r32 = lane & 31, h = lane >> 5, blk = (lane >> 4) & 1, q4 = (lane & 15) >> 2, p4 = lane & 3;

  const rsrc_t rsX = __builtin_amdgcn_make_buffer_rsrc((void*)((br ? xB : xA) + (size_t)rt * 128 * 512), 0, 128 * 1024, 0x00020000);
  const rsrc_t rsW = __builtin_amdgcn_make_buffer_rsrc((void*)(br ? wB : wA), 0, 512 * 3072, 0x00020000);
  const rsrc_t rsP = __builtin_amdgcn_make_buffer_rsrc((void*)(br ? pB : pA), 0, 32 * 512, 0x00020000);
  const rsrc_t rsO = __builtin_amdgcn_make_buffer_rsrc((void*)(br ? oB : oA), 0, 512 * 1024, 0x00020000);
  const rsrc_t rsS = __builtin_amdgcn_make_buffer_rsrc((void*)ws, 0, WS_BYTES, 0x00020000);

  const unsigned vx0 = (unsigned)((16 * w + (lane >> 3)) * 1024 + (((lane & 7) ^ (lane >> 4)) << 4));
  const unsigned vx1 = (unsigned)((16 * w + 8 + (lane >> 3)) * 1024 + (((lane & 7) ^ (4 + (lane >> 4))) << 4));
  const unsigned vw = (unsigned)((8 * w + (lane >> 3)) * 3072 + hg * 128 + (((lane & 7) ^ (((lane >> 4) & 1) << 2)) << 4));
  const unsigned vwo = (unsigned)((lane >> 4) * 1024 + (hg & 3) * 256 + (((lane & 15) ^ ((lane >> 4) << 2)) << 4));

  auto issue_chunk = [&](int s) {
    const unsigned c = (unsigned)((s + rot) & 7), sl = L + (s & 3) * SLOT;
    dma16(sl + w * 2048, vx0, rsX, c * 128);
    dma16(sl + w * 2048 + 1024, vx1, rsX, c * 128);
    dma16(sl + 16384 + w * 1024, vw, rsW, c * (64 * 3072) + 1024);
    dma16(sl + 24576 + w * 1024, vw, rsW, c * (64 * 3072) + 2048);
  };
  auto issue_wq = [&](int half) {
#pragma unroll
    for (int i = 0; i < 4; ++i) {
      const unsigned c = (unsigned)((4 * half + i + rot) & 7);
      dma16(L + half * SLOT + i * 8192 + w * 1024, vw, rsW, c * (64 * 3072));
    }
  };
  auto issue_wo = [&](int part, int slot) {
#pragma unroll
    for (int i = 0; i < 4; ++i) dma16(L + slot * SLOT + (w * 4 + i) * 1024, vwo, rsO, (unsigned)((128 * part + 4 * (w * 4 + i)) * 1024));
  };

  asm volatile("s_nop 4" ::: "memory");
  dma4(L + L_PHIW + w * 256, (unsigned)(lane * 4), rsP, (unsigned)(hg * 2048 + w * 256));
  issue_chunk(0); issue_chunk(1); issue_chunk(2); issue_chunk(3);
  const unsigned lid = (unsigned)((br * 16 + rt) * 8 + hg);
  const unsigned kvp_own = WS_KVP + (unsigned)((br * 8 + hg) * 16 + rt) * KVP_STR;
  const unsigned of_own = WS_OF + (unsigned)(((br * 64 + 4 * rt + tg) * 32 + 4 * hg + 2 * hp) * 1024);
  {
    const u32x4 poison = {0xFFFFFFFFu, 0xFFFFFFFFu, 0xFFFFFFFFu, 0xFFFFFFFFu};
    st16_sc1(poison, (unsigned)(lane * 16), rsS, of_own);
    st16_sc1(poison, (unsigned)(lane * 16), rsS, of_own + 1024);
    st16_sc1(poison, lane < 34 ? (unsigned)(lane * 16) : 0x80000000u, rsS, kvp_own + w * 544);
  }
  const u64 tag = *(const __attribute__((address_space(4))) u64*)(size_t)(ws + WS_EPOCH + br * 128) + 1;
  if (tid < 2) *(LAS u32x4*)(L + L_ZERO + tid * 16) = tid ? u32x4{0x3F803F80u, 0x3F803F80u, 0x3F803F80u, 0x3F803F80u} : u32x4{0, 0, 0, 0};

  STAMP(1);
  bf16x8 xf[32];
  f32x16 accK, accV;
#pragma unroll
  for (int i = 0; i < 16; ++i) { accK[i] = 0.f; accV[i] = 0.f; }
  const unsigned ax = L + (32 * tg + r32) * 128;
  const unsigned fx = (unsigned)(h ^ ((r32 >> 1) & 7));
  const unsigned aw = L + (8 * h + q4) * 128 + (((4 * hp + 2 * blk + (p4 >> 1)) ^ ((q4 >> 1) << 2)) << 4) + 8 * (p4 & 1);
  unsigned aw2 = aw + 2 * SLOT; asm volatile("" : "+v"(aw2));
  unsigned axk[2][4];
#pragma unroll
  for (int ks = 0; ks < 4; ++ks) { axk[0][ks] = ax + ((fx ^ (2 * ks)) << 4); axk[1][ks] = axk[0][ks] + 2 * SLOT; asm volatile("" : "+v"(axk[1][ks])); }
#pragma unroll
  for (int s = 0; s < 8; ++s) {
    if (s == 0) WAITV(15); else if (s < 4) WAITV(11); else WAITV(8);
    BAR();
    if (s == 0) STAMP(2); if (s == 1) STAMP(3); if (s == 2) STAMP(4); if (s == 3) STAMP(5); if (s == 4) STAMP(6); if (s == 5) STAMP(7); if (s == 6) STAMP(8); if (s == 7) STAMP(9);
    if (s >= 1 && s <= 4) issue_chunk(s + 3);
    if (s == 4 && tid == 0) __hip_atomic_store((GAS u64*)(ws + WS_FLAG + lid * 128), tag, __ATOMIC_RELAXED, __HIP_MEMORY_SCOPE_AGENT);
    if (s == 5) issue_wq(0);
    if (s == 6) issue_wq(1);
    if (s == 7) issue_wo(0, 2);
    const unsigned sl = (s & 1) * SLOT;
    const unsigned awb = (s & 2) ? aw2 : aw;
#pragma unroll
    for (int ks = 0; ks < 4; ++ks) {
      xf[4 * s + ks] = lds_b128(axk[(s >> 1) & 1][ks] + sl);
      bf16x8 wk = lds_tr(awb + sl + 16384 + ks * 2048, awb + sl + 16384 + ks * 2048 + 512);
      bf16x8 wv = lds_tr(awb + sl + 24576 + ks * 2048, awb + sl + 24576 + ks * 2048 + 512);
      accK = mfma32(wk, xf[4 * s + ks], accK);
      accV = mfma32(xf[4 * s + ks], wv, accV);
    }
  }
  STAMP(10);
  bf16x8 ap[2];
#pragma unroll
  for (int s = 0; s < 2; ++s) {
    const unsigned base = L + L_PHIW + (2 * hp + s) * 512 + (4 * h + q4) * 32 + (2 * blk + (p4 >> 1)) * 8;
    const bool on = (p4 & 1) == s;
    ap[s] = lds_tr(on ? base : L + L_ZERO, on ? base + 256 : L + L_ZERO);
  }
  *(LAS bf16x8*)(L + L_V + ((hp * 4 + tg) * 2 + 0) * 1024 + lane * 16) = cvt8<0>(accV);
  *(LAS bf16x8*)(L + L_V + ((hp * 4 + tg) * 2 + 1) * 1024 + lane * 16) = cvt8<8>(accV);
  BAR();
  STAMP(11);
  u32x2 fl;
  const unsigned flo = WS_FLAG + (lane < 16 ? (unsigned)((br * 16 + lane) * 8 + hg) : (unsigned)((br * 16 + rt) * 8 + (lane & 7))) * 128;
  if (w == 7) asm volatile("buffer_load_dwordx2 %0, %1, %2, 0 offen sc1" : "=v"(fl) : "v"(flo), "s"(rsS) : "memory");
  {
    f32x16 pk;
#pragma unroll
    for (int i = 0; i < 16; ++i) pk[i] = 0.f;
    pk = mfma32(ap[0], cvt8<0>(accK), pk);
    pk = mfma32(ap[1], cvt8<8>(accK), pk);
    bf16x8 f[4];
    dual_softmax(pk, f);
    const unsigned tok = 32 * tg + r32;
    const unsigned pb = L + 3 * SLOT + (2 * hp + h) * 8192 + tok * 64;
#pragma unroll
    for (int c = 0; c < 4; ++c) *(LAS bf16x8*)(pb + ((c ^ ((tok >> 1) & 3)) << 4)) = f[c];
  }
  STAMP(12);
  if (w == 7) {
    WAITV(0);
    asm volatile("" : "+v"(fl));
    unsigned spins = 0;
    while (!__all((((u64)fl.y << 32) | fl.x) == tag) && ++spins < SPIN_CAP) {
      asm volatile("buffer_load_dwordx2 %0, %1, %2, 0 offen sc1\n\ts_waitcnt vmcnt(0)" : "=&v"(fl) : "v"(flo), "s"(rsS) : "memory");
    }
  }
  WAITV(4);
  STAMP(13);
  BAR();
  STAMP(14);
  if (w < 4) {
    __builtin_amdgcn_s_setprio(2);
    const int par = w & 1;
    unsigned aa = L + 3 * SLOT + w * 8192 + (4 * h + q4) * 64 + (((2 * blk + (p4 & 1)) ^ ((2 * h + (q4 >> 1)) & 3)) << 4) + 8 * (p4 >> 1);
    asm volatile("" : "+v"(aa));
    const bool isv = blk == par;
    const unsigned vb = L + L_V + (w >> 1) * 8192 + lane * 16;
    f32x16 y;
#pragma unroll
    for (int i = 0; i < 16; ++i) y[i] = 0.f;
#pragma unroll
    for (int k2 = 0; k2 < 8; ++k2) {
      bf16x8 a = lds_tr(aa + k2 * 1024, aa + k2 * 1024 + 512);
      bf16x8 bb = lds_b128(isv ? vb + k2 * 1024 : L + L_ONES);
      y = mfma32(a, bb, y);
    }
    const unsigned so = isv ? (unsigned)((w * 4 + h) * 256 + (lane & 15) * 16) : ((lane & 15) == 0 ? (unsigned)(4096 + (w * 4 + h) * 16) : 0x80000000u);
    const unsigned ss = isv ? 512u : 32u;
    st16_sc1(nopoison(cvt8<0>(y)), so, rsS, kvp_own);
    st16_sc1(nopoison(cvt8<8>(y)), so + ss, rsS, kvp_own);
    WAITL0();
    STAMP(15);
#pragma unroll
    for (int i = 0; i < 8; ++i) dma16(L + 3 * SLOT + (w * 8 + i) * 1024, vwo, rsO, (unsigned)((128 + 4 * (w * 8 + i)) * 1024));
    __builtin_amdgcn_s_setprio(0);
  }
  STAMP(16);
  f32x16 accQ;
#pragma unroll
  for (int i = 0; i < 16; ++i) accQ[i] = 0.f;
#pragma unroll
  for (int s = 0; s < 32; ++s) {
    bf16x8 wq = lds_tr(aw + s * 2048, aw + s * 2048 + 512);
    accQ = mfma32(wq, xf[s], accQ);
  }
  STAMP(17);
  WAITV(0);
  STAMP(18);
  BAR();
  STAMP(19);
  u32x4 g[8], gz[4];
  const unsigned kvp_grp = WS_KVP + (unsigned)((br * 8 + hg) * 16) * KVP_STR;
  const unsigned gv = (unsigned)((lane >> 4) * KVP_STR + (lane & 15) * 16);
  auto gather_kv = [&]() {
#pragma unroll
    for (int i = 0; i < 8; ++i) ld16_sc1(g[i], gv, rsS, kvp_grp + (i & 3) * 4 * KVP_STR + (2 * w + (i >> 2)) * 256);
    if (w == 7) {
#pragma unroll
      for (int i = 0; i < 4; ++i) ld16_sc1(gz[i], gv, rsS, kvp_grp + i * 4 * KVP_STR + 4096);
    }
  };
  gather_kv();
  STAMP(20);
  issue_wo(2, 0); issue_wo(3, 1);
  STAMP(21);
  bf16x8 fq[4];
  {
    f32x16 pq;
#pragma unroll
    for (int i = 0; i < 16; ++i) pq[i] = 0.f;
    pq = mfma32(ap[0], cvt8<0>(accQ), pq);
    pq = mfma32(ap[1], cvt8<8>(accQ), pq);
    bf16x8 f[4];
    dual_softmax(pq, f);
#pragma unroll
    for (int sg = 0; sg < 2; ++sg) {
      u32x4 a = __builtin_bit_cast(u32x4, f[2 * sg]), c = __builtin_bit_cast(u32x4, f[2 * sg + 1]);
#pragma unroll
      for (int i = 0; i < 4; ++i) { auto r = __builtin_amdgcn_permlane32_swap(a[i], c[i], false, false); a[i] = r[0]; c[i] = r[1]; }
      fq[sg] = __builtin_bit_cast(bf16x8, a);
      fq[2 + sg] = __builtin_bit_cast(bf16x8, c);
    }
  }
  asm volatile("" :: "v"(fq[0]), "v"(fq[1]), "v"(fq[2]), "v"(fq[3]));
  STAMP(22);
  const int tw = w & 1, cw = w >> 1;
  bf16x8 wf[32];
  const unsigned awo = L + (4 * h + q4) * 256 + (((4 * cw + 2 * (p4 & 1) + blk) ^ (q4 << 2)) << 4) + 8 * (p4 >> 1);
  unsigned awo2 = awo + 2 * SLOT; asm volatile("" : "+v"(awo2));
#pragma unroll
  for (int hd = 0; hd < 16; ++hd) wf[hd] = lds_tr(awo2 + hd * 4096, awo2 + hd * 4096 + 2048);
  STAMP(23);
  {
    bf16x8 sel[2];
#pragma unroll
    for (int u = 0; u < 2; ++u)
#pragma unroll
      for (int j = 0; j < 8; ++j) sel[u][j] = ((lane & 15) == 8 * u + j) ? (__bf16)1.0f : (__bf16)0.0f;
    WAITV(8);
    STAMP(24);
    unsigned spins = 0;
    for (;;) {
#pragma unroll
      for (int i = 0; i < 8; ++i) asm volatile("" : "+v"(g[i]));
      unsigned m = 0;
#pragma unroll
      for (int i = 0; i < 8; ++i) m = max(m, umax4(g[i]));
      if (w == 7) {
#pragma unroll
        for (int i = 0; i < 4; ++i) { asm volatile("" : "+v"(gz[i])); m = max(m, umax4(gz[i])); }
      }
      if (__all(m != 0xFFFFFFFFu) || ++spins >= SPIN_CAP) break;
      gather_kv();
      WAITV(0);
    }
    STAMPV(62, spins);
    f32x4 d = {0.f, 0.f, 0.f, 0.f};
#pragma unroll
    for (int i = 0; i < 8; ++i) d = mfma16(sel[i >> 2], __builtin_bit_cast(bf16x8, g[i]), d);
    bf16x4 o4 = {(__bf16)d[0], (__bf16)d[1], (__bf16)d[2], (__bf16)d[3]};
    *(LAS bf16x4*)(L + L_KVF + (2 * w + (lane >> 5)) * 272 + (lane & 15) * 16 + ((lane >> 4) & 1) * 8) = o4;
    if (w == 7) {
      f32x4 dz = {0.f, 0.f, 0.f, 0.f};
#pragma unroll
      for (int i = 0; i < 4; ++i) dz = mfma16(sel[0], __builtin_bit_cast(bf16x8, gz[i]), dz);
      bf16x4 z4 = {(__bf16)dz[0], (__bf16)dz[1], (__bf16)dz[2], (__bf16)dz[3]};
      if (lane < 32) *(LAS bf16x4*)(L + L_KVF + (lane & 15) * 272 + 256 + (lane >> 4) * 8) = z4;
    }
  }
  STAMP(25);
  BAR();
  STAMP(26);
  {
    const unsigned kb = L + L_KVF + (8 * hp + h) * 272 + (r32 < 16 ? r32 : 16) * 16;
#pragma unroll
    for (int par = 0; par < 2; ++par) {
      f32x16 n;
#pragma unroll
      for (int i = 0; i < 16; ++i) n[i] = 0.f;
      n = mfma32(lds_b128(kb + (par * 4) * 272), fq[2 * par], n);
      n = mfma32(lds_b128(kb + (par * 4 + 2) * 272), fq[2 * par + 1], n);
      const float rd = __builtin_amdgcn_rcpf(n[8] + 1e-6f);
      bf16x8 o;
#pragma unroll
      for (int i = 0; i < 8; ++i) o[i] = (__bf16)(n[i] * rd);
      st16_sc1(nopoison(o), (unsigned)(lane * 16), rsS, of_own + par * 1024);
    }
  }
  STAMP(27);
  WAITV(0);
  STAMP(28);
  BAR();
  STAMP(29);
  const unsigned of_src = WS_OF + (unsigned)(((br * 64 + 4 * rt + 2 * (hg >> 2)) * 32 + 4 * w) * 1024);
#pragma unroll
  for (int m = 0; m < 8; ++m) ld16_sc1(g[m], (unsigned)(lane * 16), rsS, of_src + (m & 1) * 32768 + (m >> 1) * 1024);
  STAMP(30);
#pragma unroll
  for (int hd = 16; hd < 32; ++hd) wf[hd] = lds_tr(awo + (hd - 16) * 4096, awo + (hd - 16) * 4096 + 2048);
  STAMP(31);
  f32x16 acc;
#pragma unroll
  for (int i = 0; i < 16; ++i) acc[i] = 0.f;
  unsigned spins2 = 0;
  unsigned aor = L + 2 * SLOT + tw * 1024 + lane * 16; asm volatile("" : "+v"(aor));
  unsigned aow = L + 2 * SLOT + w * 8192 + lane * 16; asm volatile("" : "+v"(aow));
#pragma unroll
  for (int st = 0; st < 4; ++st) {
    if (st == 0) WAITV(6); else if (st == 1) WAITV(4); else if (st == 2) WAITV(2); else WAITV(0);
    if (st == 0) STAMP(32);
    for (;;) {
      asm volatile("" : "+v"(g[2 * st]), "+v"(g[2 * st + 1]));
      const unsigned m = max(umax4(g[2 * st]), umax4(g[2 * st + 1]));
      if (__all(m != 0xFFFFFFFFu) || ++spins2 >= SPIN_CAP) break;
      ld16_sc1(g[2 * st], (unsigned)(lane * 16), rsS, of_src + st * 1024);
      ld16_sc1(g[2 * st + 1], (unsigned)(lane * 16), rsS, of_src + 32768 + st * 1024);
      WAITV(0);
    }
    *(LAS u32x4*)(aow + st * 2048) = g[2 * st];
    *(LAS u32x4*)(aow + st * 2048 + 1024) = g[2 * st + 1];
    BAR();
    if (st == 0) STAMP(33); if (st == 1) STAMP(34); if (st == 2) STAMP(35); if (st == 3) STAMP(36);
#pragma unroll
    for (int pw = 0; pw < 8; ++pw) {
      const int hd = 4 * pw + st;
      acc = mfma32(wf[hd], lds_b128(aor + hd * 2048), acc);
    }
  }
  STAMPV(63, spins2);
  STAMP(37);
  {
    const unsigned sc = L + L_V + w * 2048;
    *(LAS bf16x8*)(sc + r32 * 64 + (((2 * h) ^ ((r32 >> 1) & 3)) << 4)) = cvt8<0>(acc);
    *(LAS bf16x8*)(sc + r32 * 64 + (((2 * h + 1) ^ ((r32 >> 1) & 3)) << 4)) = cvt8<8>(acc);
    WAITL0();
    u16* op = out + (size_t)(rt * 128 + (hg >> 2) * 64 + tw * 32) * 1024 + br * 512 + (hg & 3) * 128 + cw * 32 + (lane & 3) * 8;
#pragma unroll
    for (int i = 0; i < 2; ++i) {
      const int row = 16 * i + (lane >> 2);
      *(GAS bf16x8*)(op + (size_t)row * 1024) = lds_b128(sc + row * 64 + (((lane & 3) ^ ((row >> 1) & 3)) << 4));
    }
  }
  if (rt == 0 && hg == 0 && tid == 0) __hip_atomic_store((GAS u64*)(ws + WS_EPOCH + br * 128), tag, __ATOMIC_RELAXED, __HIP_MEMORY_SCOPE_AGENT);
}

extern "C" void kernel_launch(void* const* d_in, const int* in_sizes, int n_in, void* d_out, int out_size, void* d_ws, size_t ws_size, hipStream_t stream) {
  (void)hipFuncSetAttribute((const void*)hedgehog_fused, hipFuncAttributeMaxDynamicSharedMemorySize, L_BYTES);
  hedgehog_fused<<<256, 512, L_BYTES, stream>>>((const u16*)d_in[0], (const u16*)d_in[1], (const u16*)d_in[2], (const u16*)d_in[3],
                                                (const u16*)d_in[4], (const u16*)d_in[5], (const u16*)d_in[6], (const u16*)d_in[7],
                                                (u16*)d_out, (char*)d_ws);
}
```

```cpp
#define PROBE 0
#include <hip/hip_runtime.h>
#include <hip/hip_bf16.h>
#pragma clang diagnostic ignored "-Wint-to-pointer-cast"

typedef unsigned short u16;
typedef unsigned long long u64;
typedef __bf16 bf16x8 __attribute__((ext_vector_type(8)));
typedef __bf16 bf16x4 __attribute__((ext_vector_type(4)));
typedef __bf16 bf16x2 __attribute__((ext_vector_type(2)));
typedef float f32x16 __attribute__((ext_vector_type(16)));
typedef float f32x4 __attribute__((ext_vector_type(4)));
typedef float f32x2 __attribute__((ext_vector_type(2)));
typedef unsigned u32x4 __attribute__((ext_vector_type(4)));
typedef unsigned u32x2 __attribute__((ext_vector_type(2)));
#define LAS __attribute__((address_space(3)))
#define GAS __attribute__((address_space(1)))
#define DI __device__ __forceinline__
typedef __amdgpu_buffer_rsrc_t rsrc_t;

#define STAMPS 0

constexpr unsigned WS_EPOCH = 0;
constexpr unsigned WS_FLAG  = 4096;
constexpr unsigned WS_KVP   = 65536;
constexpr unsigned KVP_STR  = 4608;
constexpr unsigned WS_OF    = 2u << 20;
constexpr unsigned WS_BYTES = 6u << 20;
constexpr unsigned WS_STAMP = 8u << 20;
constexpr unsigned SLOT   = 32768;
constexpr unsigned L_SCR  = 131072;
constexpr unsigned SCR    = 6144;
constexpr unsigned L_PHIW = L_SCR + 4 * SCR;
constexpr unsigned L_ONES = L_PHIW + 2048;
constexpr unsigned L_ZERO = L_ONES + 2048;
constexpr unsigned L_CNT  = L_ZERO + 16;
constexpr unsigned L_TAG  = L_CNT + 16;
constexpr unsigned L_BYTES = 163840;
constexpr unsigned SPIN_CAP = 1u << 16;

#define WAITV(n) asm volatile("s_waitcnt vmcnt(" #n ")" ::: "memory")
#define WAITL0() asm volatile("s_waitcnt lgkmcnt(0)" ::: "memory")
#define BAR() do { WAITL0(); __builtin_amdgcn_s_barrier(); asm volatile("" ::: "memory"); } while (0)

DI void dma16(unsigned lds, unsigned voff, rsrc_t rs, unsigned soff) {
  asm volatile("s_mov_b32 m0, %0\n\ts_nop 0\n\tbuffer_load_dwordx4 %1, %2, %3 offen lds" :: "s"(lds), "v"(voff), "s"(rs), "s"(soff) : "memory");
}
DI void dma4(unsigned lds, unsigned voff, rsrc_t rs, unsigned soff) {
  asm volatile("s_mov_b32 m0, %0\n\ts_nop 0\n\tbuffer_load_dword %1, %2, %3 offen lds" :: "s"(lds), "v"(voff), "s"(rs), "s"(soff) : "memory");
}
DI void st16_sc1(u32x4 v, unsigned voff, rsrc_t rs, unsigned soff) {
  asm volatile("buffer_store_dwordx4 %0, %1, %2, %3 offen sc1\n\ts_nop 1" :: "v"(v), "v"(voff), "s"(rs), "s"(soff) : "memory");
}
DI void ld16_sc1(u32x4& v, unsigned voff, rsrc_t rs, unsigned soff) {
  asm volatile("buffer_load_dwordx4 %0, %1, %2, %3 offen sc1" : "=v"(v) : "v"(voff), "s"(rs), "s"(soff) : "memory");
}
DI bf16x8 lds_b128(unsigned a) { return *(const LAS bf16x8*)a; }
DI bf16x8 lds_tr(unsigned a0, unsigned a1) {
  bf16x4 lo = __builtin_amdgcn_ds_read_tr16_b64_v4bf16((LAS bf16x4*)a0);
  bf16x4 hi = __builtin_amdgcn_ds_read_tr16_b64_v4bf16((LAS bf16x4*)a1);
  return __builtin_shufflevector(lo, hi, 0, 1, 2, 3, 4, 5, 6, 7);
}
DI f32x16 mfma32(bf16x8 a, bf16x8 b, f32x16 c) { return __builtin_amdgcn_mfma_f32_32x32x16_bf16(a, b, c, 0, 0, 0); }
DI f32x4 mfma16(bf16x8 a, bf16x8 b, f32x4 c) { return __builtin_amdgcn_mfma_f32_16x16x32_bf16(a, b, c, 0, 0, 0); }
template <int O> DI bf16x8 cvt8(const f32x16& c) {
  bf16x8 r;
#pragma unroll
  for (int i = 0; i < 8; ++i) r[i] = (__bf16)c[O + i];
  return r;
}
DI u32x4 nopoison(bf16x8 v) {
  u32x4 u = __builtin_bit_cast(u32x4, v);
#pragma unroll
  for (int i = 0; i < 4; ++i) u[i] = u[i] < 0xFFFFFFFEu ? u[i] : 0xFFFFFFFEu;
  return u;
}
DI unsigned umax4(u32x4 a) { return max(max(a[0], a[1]), max(a[2], a[3])); }

DI void dual_softmax(const f32x16& p, bf16x8 (&f)[4]) {
  float mx = __builtin_fmaxf(p[0], p[1]), mn = __builtin_fminf(p[0], p[1]);
#pragma unroll
  for (int i = 2; i < 16; i += 2) { mx = __builtin_fmaxf(__builtin_fmaxf(mx, p[i]), p[i + 1]); mn = __builtin_fminf(__builtin_fminf(mn, p[i]), p[i + 1]); }
  const float L2E = 1.4426950408889634f;
  const float a = -mx * L2E, b = mn * L2E;
  f32x2 e[8], g[8];
  const f32x2 c2 = {L2E, L2E}, a2 = {a, a}, b2 = {b, b};
#pragma unroll
  for (int i = 0; i < 8; ++i) {
    f32x2 v = {p[2 * i], p[2 * i + 1]};
    f32x2 x = v * c2 + a2, y = b2 - v * c2;
    e[i] = f32x2{__builtin_amdgcn_exp2f(x.x), __builtin_amdgcn_exp2f(x.y)};
    g[i] = f32x2{__builtin_amdgcn_exp2f(y.x), __builtin_amdgcn_exp2f(y.y)};
  }
  f32x2 se = ((e[0] + e[1]) + (e[2] + e[3])) + ((e[4] + e[5]) + (e[6] + e[7]));
  f32x2 sg = ((g[0] + g[1]) + (g[2] + g[3])) + ((g[4] + g[5]) + (g[6] + g[7]));
  const float re = __builtin_amdgcn_rcpf(se.x + se.y), rg = __builtin_amdgcn_rcpf(sg.x + sg.y);
  const f32x2 re2 = {re, re}, rg2 = {rg, rg};
#pragma unroll
  for (int i = 0; i < 8; ++i) {
    f32x2 x = e[i] * re2, y = g[i] * rg2;
    f[i >> 2][2 * (i & 3)] = (__bf16)x.x; f[i >> 2][2 * (i & 3) + 1] = (__bf16)x.y;
    f[2 + (i >> 2)][2 * (i & 3)] = (__bf16)y.x; f[2 + (i >> 2)][2 * (i & 3) + 1] = (__bf16)y.y;
  }
}

#define STAMP(i) do { if ((i) == PROBE) asm volatile("s_memrealtime %0\n\ts_waitcnt lgkmcnt(0)" : "=s"(probe_t) :: "memory"); } while (0)
#define STAMPR(i) do {} while (0)
#define STAMPV(i, x) do {} while (0)

#define SB() __builtin_amdgcn_sched_barrier(0)
DI void cnt_arrive(unsigned a) {
  asm volatile("s_waitcnt lgkmcnt(0)" ::: "memory");
  if ((threadIdx.x & 63) == 0) __hip_atomic_fetch_add((LAS unsigned*)a, 1u, __ATOMIC_RELAXED, __HIP_MEMORY_SCOPE_WORKGROUP);
}
DI void cnt_wait(unsigned a, unsigned n) {
  unsigned v;
  do { asm volatile("ds_read_b32 %0, %1\n\ts_waitcnt lgkmcnt(0)" : "=&v"(v) : "v"(a) : "memory"); } while ((unsigned)__builtin_amdgcn_readfirstlane(v) < n);
}
DI f32x16 zero16() { f32x16 z;
#pragma unroll
  for (int i = 0; i < 16; ++i) z[i] = 0.f;
  return z; }
DI bool has_poison(unsigned m) { return !__all(m != 0xFFFFFFFFu); }

__global__ __launch_bounds__(512, 2) void hedgehog_fused(
    const u16* __restrict__ xA, const u16* __restrict__ wA, const u16* __restrict__ pA, const u16* __restrict__ oA,
    const u16* __restrict__ xB, const u16* __restrict__ wB, const u16* __restrict__ pB, const u16* __restrict__ oB,
    u16* __restrict__ out, char* __restrict__ ws) {
  extern __shared__ __attribute__((aligned(16))) char smem[];
  u64 probe_t = 0;
  STAMPR(60); STAMP(0);
  const unsigned L = (unsigned)(size_t)(LAS char*)smem;
  const int b = blockIdx.x, xcd = b & 7, jj = b >> 3;
  const int br = xcd >> 2, rt = (xcd & 3) * 4 + (jj >> 3), hg = jj & 7;
  const int rot = (hg + 2 * (rt & 3)) & 7;
  const int tid = threadIdx.x, lane = tid & 63;
  const int w = __builtin_amdgcn_readfirstlane(tid >> 6);
  const int u = w & 3;
  const int r32 = lane & 31, h = lane >> 5, blk = (lane >> 4) & 1, q4 = (lane & 15) >> 2, p4 = lane & 3;
  const rsrc_t rsO = __builtin_amdgcn_make_buffer_rsrc((void*)(br ? oB : oA), 0, 512 * 1024, 0x00020000);
  const rsrc_t rsS = __builtin_amdgcn_make_buffer_rsrc((void*)ws, 0, WS_BYTES, 0x00020000);
  const unsigned lid = (unsigned)((br * 16 + rt) * 8 + hg);
  const unsigned vwo = (unsigned)((lane >> 4) * 1024 + (hg & 3) * 256 + (((lane & 15) ^ ((lane >> 4) << 2)) << 4));
  const u32x4 poison = {0xFFFFFFFFu, 0xFFFFFFFFu, 0xFFFFFFFFu, 0xFFFFFFFFu};
  auto issue_wo = [&](int part, int slot, int i0, int i1) {
    const unsigned so = (unsigned)((64 * ((hg + 2 * part + (u >> 1)) & 7) + 32 * (u & 1)) * 1024);
#pragma unroll
    for (int i = i0; i < i1; ++i) dma16(L + slot * SLOT + (u * 8 + i) * 1024, vwo, rsO, so + i * 4096);
  };
  const unsigned fx = (unsigned)(h ^ ((r32 >> 1) & 7));

  if (w >= 4) {
    const rsrc_t rsX = __builtin_amdgcn_make_buffer_rsrc((void*)((br ? xB : xA) + (size_t)rt * 128 * 512), 0, 128 * 1024, 0x00020000);
    const rsrc_t rsW = __builtin_amdgcn_make_buffer_rsrc((void*)(br ? wB : wA), 0, 512 * 3072, 0x00020000);
    const unsigned vxe = (unsigned)((lane >> 3) * 1024 + (((lane & 7) ^ (lane >> 4)) << 4));
    const unsigned vxo = vxe ^ 64u;
    const unsigned vwk = (unsigned)((lane >> 3) * 3072 + hg * 128 + (((lane & 7) ^ (((lane >> 4) & 1) << 2)) << 4));
    const unsigned vwv = vwk ^ 32u;
    auto issue = [&](int v, int i0, int i1) {
      if (v < 8) {
        const unsigned c = (unsigned)((v + rot) & 7), sl = L + (v & 3) * SLOT;
        const unsigned sx = c * 128 + u * 32768, sw = c * (64 * 3072) + u * (16 * 3072);
#pragma unroll
        for (int i = i0; i < i1; ++i) {
          if (i < 2) dma16(sl + 16384 + (2 * u + i) * 1024, vwk, rsW, sw + i * (8 * 3072) + 1024);
          else if (i < 4) dma16(sl + 24576 + (2 * u + i - 2) * 1024, vwv, rsW, sw + (i - 2) * (8 * 3072) + 2048);
          else dma16(sl + (4 * u + i - 4) * 1024, (i & 1) ? vxo : vxe, rsX, sx + (i - 4) * 8192);
        }
      } else if (v < 10) {
#pragma unroll
        for (int i = i0; i < i1; ++i) {
          const unsigned c = (unsigned)((4 * (v - 8) + (i >> 1) + rot) & 7);
          dma16(L + (v - 8) * SLOT + (i >> 1) * 8192 + (2 * u + (i & 1)) * 1024, vwk, rsW, c * (64 * 3072) + (2 * u + (i & 1)) * (8 * 3072));
        }
      } else issue_wo(0, 2, i0, i1);
    };
    asm volatile("s_nop 4" ::: "memory");
    STAMP(50);
    issue(0, 0, 8);
    STAMP(51);
    issue(1, 0, 8);
    bf16x8 xf[32];
    unsigned axk[2][4];
#pragma unroll
    for (int ks = 0; ks < 4; ++ks) { axk[0][ks] = L + (32 * u + r32) * 128 + ((fx ^ (2 * ks)) << 4); axk[1][ks] = axk[0][ks] + 2 * SLOT; asm volatile("" : "+v"(axk[1][ks])); }
#pragma unroll
    for (int s = 0; s < 8; ++s) {
      issue(s + 2, 0, 3);
      WAITV(11);
      BAR();
      if (s == 0) STAMP(2); if (s == 1) STAMP(3); if (s == 2) STAMP(4); if (s == 3) STAMP(5); if (s == 4) STAMP(6); if (s == 5) STAMP(7); if (s == 6) STAMP(8); if (s == 7) STAMP(9);
      issue(s + 2, 3, 4);
#pragma unroll
      for (int ks = 0; ks < 4; ++ks) xf[4 * s + ks] = lds_b128(axk[(s >> 1) & 1][ks] + (s & 1) * SLOT);
      SB();
      issue(s + 2, 4, 8);
    }
    issue(10, 0, 8);
    STAMP(10);
    bf16x8 ap[4];
#pragma unroll
    for (int a = 0; a < 4; ++a) {
      const unsigned base = L + L_PHIW + a * 512 + (8 * h + q4) * 32 + (2 * blk + (p4 >> 1)) * 8;
      const bool on = (p4 & 1) == (a & 1);
      ap[a] = lds_tr(on ? base : L + L_ZERO, on ? base + 128 : L + L_ZERO);
    }
    WAITV(8);
    cnt_arrive(L + L_CNT);
    cnt_wait(L + L_CNT, 8);
    STAMP(11);
    issue_wo(1, 3, 0, 8);
    const unsigned of_own = WS_OF + (unsigned)(((br * 64 + 4 * rt + u) * 32 + 4 * hg) * 1024);
#pragma unroll
    for (int a = 0; a < 4; ++a) st16_sc1(poison, (unsigned)(lane * 16), rsS, of_own + a * 1024);
    STAMP(12);
    f32x16 accQ[2] = {zero16(), zero16()};
    {
      const unsigned aw0 = L + (8 * h + q4) * 128 + (((2 * blk + (p4 & 1)) ^ ((q4 >> 1) << 2)) << 4) + 8 * (p4 >> 1);
      unsigned aw1 = aw0 ^ 64u; asm volatile("" : "+v"(aw1));
      bf16x8 wq[3][2];
#pragma unroll
      for (int s = 0; s < 2; ++s) { wq[s][0] = lds_tr(aw0 + s * 2048, aw0 + s * 2048 + 512); wq[s][1] = lds_tr(aw1 + s * 2048, aw1 + s * 2048 + 512); }
      SB();
#pragma unroll
      for (int s = 0; s < 32; ++s) {
        if (s + 2 < 32) wq[(s + 2) % 3][0] = lds_tr(aw0 + (s + 2) * 2048, aw0 + (s + 2) * 2048 + 512);
        accQ[0] = mfma32(wq[s % 3][0], xf[s], accQ[0]);
        SB();
        if (s + 2 < 32) wq[(s + 2) % 3][1] = lds_tr(aw1 + (s + 2) * 2048, aw1 + (s + 2) * 2048 + 512);
        accQ[1] = mfma32(wq[s % 3][1], xf[s], accQ[1]);
        SB();
      }
    }
    WAITV(0);
    cnt_arrive(L + L_CNT + 4);
    STAMP(13);
    bf16x8 fq[4][2];
#pragma unroll
    for (int hp = 0; hp < 2; ++hp) {
      f32x16 pq = mfma32(ap[2 * hp], cvt8<0>(accQ[hp]), zero16());
      pq = mfma32(ap[2 * hp + 1], cvt8<8>(accQ[hp]), pq);
      bf16x8 f[4];
      dual_softmax(pq, f);
#pragma unroll
      for (int sg = 0; sg < 2; ++sg) {
        u32x4 a = __builtin_bit_cast(u32x4, f[2 * sg]), c = __builtin_bit_cast(u32x4, f[2 * sg + 1]);
#pragma unroll
        for (int i = 0; i < 4; ++i) { auto r = __builtin_amdgcn_permlane32_swap(a[i], c[i], false, false); a[i] = r[0]; c[i] = r[1]; }
        fq[2 * hp][sg] = __builtin_bit_cast(bf16x8, a);
        fq[2 * hp + 1][sg] = __builtin_bit_cast(bf16x8, c);
      }
    }
#pragma unroll
    for (int a = 0; a < 4; ++a) asm volatile("" : "+v"(fq[a][0]), "+v"(fq[a][1]));
    const u64 tag = *(const LAS u64*)(L + L_TAG);
    u32x2 fl;
    const unsigned flo = WS_FLAG + (unsigned)((br * 16 + rt) * 8 + (lane & 7)) * 128 + 64;
    asm volatile("buffer_load_dwordx2 %0, %1, %2, 0 offen sc1" : "=v"(fl) : "v"(flo), "s"(rsS) : "memory");
    unsigned kb = L + L_SCR + h * 320 + (r32 < 16 ? r32 : 16 + (r32 & 3)) * 16; asm volatile("" : "+v"(kb));
    unsigned aown = L + L_SCR + 2048 + (u & 1) * 1024 + lane * 16; asm volatile("" : "+v"(aown));
    unsigned aow = L + 2 * SLOT + u * 2048 + lane * 16; asm volatile("" : "+v"(aow));
    STAMP(14);
    BAR();
    STAMP(26);
    {
      bf16x8 ka[4][2];
#pragma unroll
      for (int a = 0; a < 4; ++a) { ka[a][0] = lds_b128(kb + a * SCR); ka[a][1] = lds_b128(kb + a * SCR + 640); }
      f32x16 n[4];
#pragma unroll
      for (int a = 0; a < 4; ++a) n[a] = mfma32(ka[a][0], fq[a][0], zero16());
#pragma unroll
      for (int a = 0; a < 4; ++a) n[a] = mfma32(ka[a][1], fq[a][1], n[a]);
      const bool own = (u >> 1) == (hg >> 2);
#pragma unroll
      for (int a = 0; a < 4; ++a) {
        const float rd = __builtin_amdgcn_rcpf(((n[a][8] + n[a][9]) + (n[a][10] + n[a][11])) + 1e-6f);
        const f32x2 r2 = {rd, rd};
        bf16x8 o;
#pragma unroll
        for (int i = 0; i < 4; ++i) { f32x2 v = f32x2{n[a][2 * i], n[a][2 * i + 1]} * r2; o[2 * i] = (__bf16)v.x; o[2 * i + 1] = (__bf16)v.y; }
        const u32x4 ou = nopoison(o);
        st16_sc1(ou, (unsigned)(lane * 16), rsS, of_own + a * 1024);
        if (own) *(LAS u32x4*)(aown + a * SCR) = ou;
      }
    }
    STAMP(27);
    BAR();
    STAMP(28);
    WAITV(0);
    asm volatile("" : "+v"(fl));
    {
      unsigned spins = 0;
      while (!__all((((u64)fl.y << 32) | fl.x) == tag) && ++spins < SPIN_CAP)
        asm volatile("buffer_load_dwordx2 %0, %1, %2, 0 offen sc1\n\ts_waitcnt vmcnt(0)" : "=&v"(fl) : "v"(flo), "s"(rsS) : "memory");
    }
    STAMP(29);
    const unsigned of_src = WS_OF + (unsigned)(((br * 64 + 4 * rt + 2 * (hg >> 2)) * 32 + u) * 1024);
    u32x4 g[8][2];
    auto gload = [&](int st) {
      const unsigned so = of_src + (unsigned)(((hg + st) & 7) * 4096);
      ld16_sc1(g[st][0], (unsigned)(lane * 16), rsS, so);
      ld16_sc1(g[st][1], (unsigned)(lane * 16), rsS, so + 32768);
    };
    gload(1); gload(2); gload(3); gload(4);
    STAMP(30);
    unsigned spins2 = 0;
#pragma unroll
    for (int st = 1; st < 8; ++st) {
      if (st <= 4) WAITV(6); else if (st == 5) WAITV(4); else if (st == 6) WAITV(2); else WAITV(0);
      if (st == 1) STAMP(31);
      for (;;) {
        asm volatile("" : "+v"(g[st][0]), "+v"(g[st][1]));
        if (!has_poison(max(umax4(g[st][0]), umax4(g[st][1]))) || ++spins2 >= SPIN_CAP) break;
        gload(st);
        WAITV(0);
      }
      *(LAS u32x4*)(aow + (st - 1) * 8192) = g[st][0];
      *(LAS u32x4*)(aow + (st - 1) * 8192 + 1024) = g[st][1];
      if (st + 4 < 8) gload(st + 4);
      BAR();
      if (st == 1) STAMP(32); if (st == 2) STAMP(33); if (st == 3) STAMP(34); if (st == 4) STAMP(35); if (st == 5) STAMP(36); if (st == 6) STAMP(37); if (st == 7) STAMP(38);
    }
    STAMPV(63, spins2);
    if (rt == 0 && hg == 0 && tid == 256) __hip_atomic_store((GAS u64*)(ws + WS_EPOCH + br * 128), tag, __ATOMIC_RELAXED, __HIP_MEMORY_SCOPE_AGENT);
  } else {
    const unsigned kvp_own = WS_KVP + (unsigned)((br * 8 + hg) * 16 + rt) * KVP_STR;
    {
      const rsrc_t rsP = __builtin_amdgcn_make_buffer_rsrc((void*)(br ? pB : pA), 0, 32 * 512, 0x00020000);
      asm volatile("s_nop 4" ::: "memory");
      if (u < 2) dma16(L + L_PHIW + u * 1024, (unsigned)(lane * 16), rsP, (unsigned)(hg * 2048 + u * 1024));
      st16_sc1(poison, (unsigned)(lane * 16), rsS, kvp_own + u * 1024);
      st16_sc1(poison, lane < 4 ? (unsigned)(lane * 16) : 0x80000000u, rsS, kvp_own + 4096 + u * 64);
      if (u >= 2) *(LAS u32x4*)(L + L_ONES + (u - 2) * 1024 + lane * 16) = u32x4{0x3F803F80u, 0x3F803F80u, 0x3F803F80u, 0x3F803F80u};
      if (u == 0 && lane < 2) *(LAS u32x4*)(L + L_ZERO + lane * 16) = u32x4{0, 0, 0, 0};
    }
    u64 tag;
    asm volatile("s_load_dwordx2 %0, %1, 0x0\n\ts_waitcnt lgkmcnt(0)" : "=&s"(tag) : "s"(ws + WS_EPOCH + br * 128) : "memory");
    tag += 1;
    if (tid == 0) *(LAS u64*)(L + L_TAG) = tag;
    f32x16 acc[4] = {zero16(), zero16(), zero16(), zero16()};
    unsigned axk[2][4];
#pragma unroll
    for (int ks = 0; ks < 4; ++ks) { axk[0][ks] = L + r32 * 128 + ((fx ^ (2 * ks)) << 4); axk[1][ks] = axk[0][ks] + 2 * SLOT; asm volatile("" : "+v"(axk[0][ks]), "+v"(axk[1][ks])); }
    unsigned awb[2];
    awb[0] = L + 16384 + blk * 8192 + (8 * h + q4) * 128 + (((2 * u + (p4 & 1)) ^ ((q4 >> 1) << 2) ^ (blk << 1)) << 4) + 8 * (p4 >> 1);
    awb[1] = awb[0] + 2 * SLOT; asm volatile("" : "+v"(awb[0]), "+v"(awb[1]));
    bf16x8 fa[3], fb[3][4];
    STAMP(1);
#pragma unroll
    for (int s = 0; s < 8; ++s) {
      if (s == 2) WAITV(0);
      BAR();
      if (s == 0) STAMP(2); if (s == 1) STAMP(3); if (s == 2) STAMP(4); if (s == 3) STAMP(5); if (s == 4) STAMP(6); if (s == 5) STAMP(7); if (s == 6) STAMP(8); if (s == 7) STAMP(9);
      if (s == 2 && tid == 0) __hip_atomic_store((GAS u64*)(ws + WS_FLAG + lid * 128), tag, __ATOMIC_RELAXED, __HIP_MEMORY_SCOPE_AGENT);
      const unsigned sl = (s & 1) * SLOT;
#define RD_A(ks) fa[(4 * s + (ks)) % 3] = lds_tr(awb[(s >> 1) & 1] + sl + (ks) * 2048, awb[(s >> 1) & 1] + sl + (ks) * 2048 + 512)
#define RD_B(ks, t) fb[(4 * s + (ks)) % 3][t] = lds_b128(axk[(s >> 1) & 1][ks] + sl + (t) * 4096)
#define MM(n, t) acc[t] = mfma32(fa[(n) % 3], fb[(n) % 3][t], acc[t])
      SB();
      if (s > 0) {
        MM(4 * s - 1, 0); SB(); RD_A(0); RD_B(0, 0); SB();
        MM(4 * s - 1, 1); SB(); RD_B(0, 1); RD_B(0, 2); RD_B(0, 3); SB();
        MM(4 * s - 1, 2); SB(); RD_A(1); RD_B(1, 0); SB();
        MM(4 * s - 1, 3); SB(); RD_B(1, 1); RD_B(1, 2); RD_B(1, 3); SB();
      } else {
        RD_A(0); RD_B(0, 0); RD_B(0, 1); RD_B(0, 2); RD_B(0, 3); RD_A(1); RD_B(1, 0); RD_B(1, 1); RD_B(1, 2); RD_B(1, 3); SB();
      }
      MM(4 * s, 0); SB(); RD_A(2); SB();
      MM(4 * s, 1); SB(); RD_B(2, 0); RD_B(2, 1); SB();
      MM(4 * s, 2); SB(); RD_B(2, 2); RD_B(2, 3); SB();
      MM(4 * s, 3); SB();
      MM(4 * s + 1, 0); SB(); RD_A(3); SB();
      MM(4 * s + 1, 1); SB(); RD_B(3, 0); RD_B(3, 1); SB();
      MM(4 * s + 1, 2); SB(); RD_B(3, 2); RD_B(3, 3); SB();
      MM(4 * s + 1, 3); SB();
      MM(4 * s + 2, 0); MM(4 * s + 2, 1); MM(4 * s + 2, 2); MM(4 * s + 2, 3); SB();
    }
    cnt_arrive(L + L_CNT);
    bf16x8 ap[2];
#pragma unroll
    for (int s = 0; s < 2; ++s) {
      const unsigned base = L + L_PHIW + u * 512 + (8 * h + q4) * 32 + (2 * blk + (p4 >> 1)) * 8;
      const bool on = (p4 & 1) == s;
      ap[s] = lds_tr(on ? base : L + L_ZERO, on ? base + 128 : L + L_ZERO);
    }
    u32x2 fl;
    const unsigned flo = WS_FLAG + (unsigned)((br * 16 + (lane & 15)) * 8 + hg) * 128;
    asm volatile("buffer_load_dwordx2 %0, %1, %2, 0 offen sc1" : "=v"(fl) : "v"(flo), "s"(rsS) : "memory");
    MM(31, 0); MM(31, 1); MM(31, 2); MM(31, 3);
    STAMP(10);
    __builtin_amdgcn_s_setprio(2);
    const unsigned scr = L + L_SCR + u * SCR;
    f32x16 y = zero16();
    {
      const unsigned tq = (unsigned)lane;
      const unsigned pw = scr + tq * 64, sw = (tq >> 1) & 3;
      const unsigned vw0 = scr + 4096 + r32 * 32 + ((h ^ ((r32 >> 2) & 1)) << 4);
      unsigned aa = scr + (8 * h + q4) * 64 + 8 * (p4 >> 1);
      const unsigned aa0 = aa + (((2 * blk + (p4 & 1)) ^ (q4 >> 1)) << 4), aa1 = aa + 256 + (((2 * blk + (p4 & 1)) ^ (2 + (q4 >> 1))) << 4);
      const unsigned ab = scr + 4096 + (8 * h + q4) * 32 + 8 * (p4 & 1);
      const unsigned ab0 = blk ? L + L_ONES + 128 : ab + ((p4 >> 1) << 4), ab1 = blk ? L + L_ONES + 256 : ab + 128 + (((p4 >> 1) ^ 1) << 4);
      f32x16 pk[2];
#pragma unroll
      for (int gm = 0; gm < 2; ++gm) {
        pk[gm] = mfma32(ap[0], cvt8<0>(acc[2 * gm]), zero16());
        pk[gm] = mfma32(ap[1], cvt8<0>(acc[2 * gm + 1]), pk[gm]);
      }
#pragma unroll
      for (int gm = 0; gm < 2; ++gm) {
        bf16x8 f[4];
        dual_softmax(pk[gm], f);
        *(LAS bf16x8*)(vw0) = cvt8<8>(acc[2 * gm]);
        *(LAS bf16x8*)(vw0 + 1024) = cvt8<8>(acc[2 * gm + 1]);
#pragma unroll
        for (int c = 0; c < 4; ++c) *(LAS bf16x8*)(pw + ((c ^ sw) << 4)) = f[c];
        asm volatile("" ::: "memory");
        bf16x8 ra[4], rb[4];
#pragma unroll
        for (int k2 = 0; k2 < 4; ++k2) { ra[k2] = lds_tr(aa0 + k2 * 1024, aa1 + k2 * 1024); rb[k2] = lds_tr(ab0 + k2 * 512, ab1 + k2 * 512); }
        asm volatile("" ::: "memory");
#pragma unroll
        for (int k2 = 0; k2 < 4; ++k2) y = mfma32(ra[k2], rb[k2], y);
        if (gm == 0) STAMP(15);
      }
    }
    {
      const unsigned so = blk == 0 ? (unsigned)((u * 4 + h) * 256 + (lane & 15) * 16) : ((lane & 15) == 0 ? (unsigned)(4096 + (u * 4 + h) * 16) : 0x80000000u);
      const unsigned ss = blk == 0 ? 512u : 32u;
      st16_sc1(nopoison(cvt8<0>(y)), so, rsS, kvp_own);
      st16_sc1(nopoison(cvt8<8>(y)), so + ss, rsS, kvp_own);
    }
    __builtin_amdgcn_s_setprio(0);
    STAMP(16);
    bf16x8 sel[2];
#pragma unroll
    for (int v = 0; v < 2; ++v)
#pragma unroll
      for (int j = 0; j < 8; ++j) sel[v][j] = ((lane & 15) == 8 * v + j) ? (__bf16)1.0f : (__bf16)0.0f;
    asm volatile("" : "+v"(sel[0]), "+v"(sel[1]));
    const unsigned kvp_grp = WS_KVP + (unsigned)((br * 8 + hg) * 16) * KVP_STR;
    const unsigned gv = (unsigned)((lane >> 4) * KVP_STR + (lane & 15) * 16);
    const unsigned gzv = (unsigned)((lane >> 2) * KVP_STR + (lane & 3) * 16);
    unsigned kw = scr + (lane >> 5) * 320 + (lane & 15) * 16 + ((lane >> 4) & 1) * 8; asm volatile("" : "+v"(kw));
    unsigned zw = scr + (lane & 3) * 320 + 256 + ((lane >> 2) & 3) * 16 + ((lane >> 4) & 1) * 8; asm volatile("" : "+v"(zw));
    WAITV(0);
    asm volatile("" : "+v"(fl));
    {
      unsigned spins = 0;
      while (!__all((((u64)fl.y << 32) | fl.x) == tag) && ++spins < SPIN_CAP)
        asm volatile("buffer_load_dwordx2 %0, %1, %2, 0 offen sc1\n\ts_waitcnt vmcnt(0)" : "=&v"(fl) : "v"(flo), "s"(rsS) : "memory");
    }
    STAMP(17);
    u32x4 g[16], gz;
    auto gather_kv = [&]() {
#pragma unroll
      for (int i = 0; i < 16; ++i) ld16_sc1(g[i], gv, rsS, kvp_grp + (i & 3) * 4 * KVP_STR + (4 * u + (i >> 2)) * 256);
      ld16_sc1(gz, gzv, rsS, kvp_grp + 4096 + u * 64);
    };
    gather_kv();
    STAMP(18);
    cnt_wait(L + L_CNT + 4, 4);
    if (tid == 0) __hip_atomic_store((GAS u64*)(ws + WS_FLAG + lid * 128 + 64), tag, __ATOMIC_RELAXED, __HIP_MEMORY_SCOPE_AGENT);
    STAMP(19);
    issue_wo(2, 0, 0, 8);
    STAMP(20);
    WAITV(8);
    STAMP(21);
    {
      unsigned spins = 0;
      f32x4 d0, d1, dz;
      for (;;) {
#pragma unroll
        for (int i = 0; i < 16; ++i) asm volatile("" : "+v"(g[i]));
        asm volatile("" : "+v"(gz));
        d0 = f32x4{0.f, 0.f, 0.f, 0.f}; d1 = d0;
#pragma unroll
        for (int i = 0; i < 8; ++i) { d0 = mfma16(sel[i >> 2], __builtin_bit_cast(bf16x8, g[i]), d0); d1 = mfma16(sel[i >> 2], __builtin_bit_cast(bf16x8, g[8 + i]), d1); }
        dz = mfma16(sel[0], __builtin_bit_cast(bf16x8, gz), f32x4{0.f, 0.f, 0.f, 0.f});
        const float chk = (d0[0] + d1[0]) + dz[0];
        if (__all(chk == chk)) break;
        unsigned m = umax4(gz);
#pragma unroll
        for (int i = 0; i < 16; ++i) m = max(m, umax4(g[i]));
        if (!has_poison(m) || spins >= SPIN_CAP) break;
        do {
          ld16_sc1(gz, gzv, rsS, kvp_grp + 4096 + u * 64);
          WAITV(0);
          asm volatile("" : "+v"(gz));
        } while (++spins < SPIN_CAP && has_poison(umax4(gz)));
        gather_kv();
        WAITV(0);
      }
      STAMPV(62, spins);
      *(LAS bf16x4*)(kw) = bf16x4{(__bf16)d0[0], (__bf16)d0[1], (__bf16)d0[2], (__bf16)d0[3]};
      *(LAS bf16x4*)(kw + 640) = bf16x4{(__bf16)d1[0], (__bf16)d1[1], (__bf16)d1[2], (__bf16)d1[3]};
      if (lane < 32) *(LAS bf16x4*)(zw) = bf16x4{(__bf16)dz[0], (__bf16)dz[1], (__bf16)dz[2], (__bf16)dz[3]};
    }
    STAMP(25);
    BAR();
    STAMP(26);
    issue_wo(3, 1, 0, 8);
    bf16x8 wf[32];
    const unsigned awo = L + (4 * h + q4) * 256 + (((4 * u + 2 * (p4 & 1) + blk) ^ (q4 << 2)) << 4) + 8 * (p4 >> 1);
    unsigned awo2 = awo + 2 * SLOT; asm volatile("" : "+v"(awo2));
#pragma unroll
    for (int hd = 0; hd < 16; ++hd) wf[hd] = lds_tr(awo2 + hd * 4096, awo2 + hd * 4096 + 2048);
    unsigned aor0 = L + L_SCR + 2048 + lane * 16; asm volatile("" : "+v"(aor0));
    unsigned aor = L + 2 * SLOT + lane * 16; asm volatile("" : "+v"(aor));
    STAMP(27);
    BAR();
    STAMP(28);
    f32x16 c2[2] = {zero16(), zero16()};
    bf16x8 ob[2][8];
#pragma unroll
    for (int i = 0; i < 8; ++i) ob[0][i] = lds_b128(aor0 + (i >> 1) * SCR + (i & 1) * 1024);
#pragma unroll
    for (int i = 0; i < 8; ++i) c2[i & 1] = mfma32(wf[i >> 1], ob[0][i], c2[i & 1]);
    SB();
    WAITV(0);
    cnt_arrive(L + L_CNT + 8);
    cnt_wait(L + L_CNT + 8, 4);
#pragma unroll
    for (int hd = 16; hd < 32; ++hd) wf[hd] = lds_tr(awo + (hd - 16) * 4096, awo + (hd - 16) * 4096 + 2048);
    STAMP(30);
#pragma unroll
    for (int st = 1; st < 8; ++st) {
      BAR();
      if (st == 1) STAMP(32); if (st == 2) STAMP(33); if (st == 3) STAMP(34); if (st == 4) STAMP(35); if (st == 5) STAMP(36); if (st == 6) STAMP(37); if (st == 7) STAMP(38);
      SB();
#pragma unroll
      for (int i = 0; i < 8; ++i) {
        if (st > 1) { c2[i & 1] = mfma32(wf[4 * (st - 1) + (i >> 1)], ob[(st - 1) & 1][i], c2[i & 1]); SB(); }
        if (i < 4) { ob[st & 1][2 * i] = lds_b128(aor + (st - 1) * 8192 + (2 * i) * 1024); ob[st & 1][2 * i + 1] = lds_b128(aor + (st - 1) * 8192 + (2 * i + 1) * 1024); SB(); }
      }
    }
#pragma unroll
    for (int i = 0; i < 8; ++i) c2[i & 1] = mfma32(wf[28 + (i >> 1)], ob[1][i], c2[i & 1]);
    STAMP(39);
    {
#pragma unroll
      for (int t = 0; t < 2; ++t) {
        *(LAS bf16x8*)(scr + t * 2048 + r32 * 64 + (((2 * h) ^ ((r32 >> 1) & 3)) << 4)) = cvt8<0>(c2[t]);
        *(LAS bf16x8*)(scr + t * 2048 + r32 * 64 + (((2 * h + 1) ^ ((r32 >> 1) & 3)) << 4)) = cvt8<8>(c2[t]);
      }
      asm volatile("" ::: "memory");
      u16* op = out + (size_t)(rt * 128 + (hg >> 2) * 64 + (lane >> 2)) * 1024 + br * 512 + (hg & 3) * 128 + u * 32 + (lane & 3) * 8;
      const unsigned rd = scr + (lane >> 2) * 64 + (((lane & 3) ^ ((lane >> 3) & 3)) << 4);
#pragma unroll
      for (int i = 0; i < 4; ++i) *(GAS bf16x8*)(op + (size_t)i * 16 * 1024) = lds_b128(rd + i * 1024);
    }
  }
  STAMP(40);
  WAITV(0);
  STAMP(41);
  if (probe_t) { u64 now; do { __builtin_amdgcn_s_sleep(4); asm volatile("s_memrealtime %0\n\ts_waitcnt lgkmcnt(0)" : "=s"(now) :: "memory"); } while (now < probe_t + 4000); }
}

extern "C" void kernel_launch(void* const* d_in, const int* in_sizes, int n_in, void* d_out, int out_size, void* d_ws, size_t ws_size, hipStream_t stream) {
  (void)hipFuncSetAttribute((const void*)hedgehog_fused, hipFuncAttributeMaxDynamicSharedMemorySize, L_BYTES);
  hedgehog_fused<<<256, 512, L_BYTES, stream>>>((const u16*)d_in[0], (const u16*)d_in[1], (const u16*)d_in[2], (const u16*)d_in[3],
                                                (const u16*)d_in[4], (const u16*)d_in[5], (const u16*)d_in[6], (const u16*)d_in[7],
                                                (u16*)d_out, (char*)d_ws);
}
```

```cpp
#define PROBE 0
#define OUTMODE 1
#include <hip/hip_runtime.h>
#include <hip/hip_bf16.h>
#pragma clang diagnostic ignored "-Wint-to-pointer-cast"

typedef unsigned short u16;
typedef unsigned long long u64;
typedef __bf16 bf16x8 __attribute__((ext_vector_type(8)));
typedef __bf16 bf16x4 __attribute__((ext_vector_type(4)));
typedef __bf16 bf16x2 __attribute__((ext_vector_type(2)));
typedef float f32x16 __attribute__((ext_vector_type(16)));
typedef float f32x4 __attribute__((ext_vector_type(4)));
typedef float f32x2 __attribute__((ext_vector_type(2)));
typedef unsigned u32x4 __attribute__((ext_vector_type(4)));
typedef unsigned u32x2 __attribute__((ext_vector_type(2)));
#define LAS __attribute__((address_space(3)))
#define GAS __attribute__((address_space(1)))
#define DI __device__ __forceinline__
typedef __amdgpu_buffer_rsrc_t rsrc_t;

#define STAMPS 0

constexpr unsigned WS_EPOCH = 0;
constexpr unsigned WS_FLAG  = 4096;
constexpr unsigned WS_KVP   = 65536;
constexpr unsigned KVP_STR  = 4608;
constexpr unsigned WS_OF    = 2u << 20;
constexpr unsigned WS_BYTES = 6u << 20;
constexpr unsigned WS_STAMP = 8u << 20;
constexpr unsigned SLOT   = 32768;
constexpr unsigned L_SCR  = 131072;
constexpr unsigned SCR    = 6144;
constexpr unsigned L_PHIW = L_SCR + 4 * SCR;
constexpr unsigned L_ONES = L_PHIW + 2048;
constexpr unsigned L_ZERO = L_ONES + 2048;
constexpr unsigned L_CNT  = L_ZERO + 16;
constexpr unsigned L_TAG  = L_CNT + 16;
constexpr unsigned L_BYTES = 163840;
constexpr unsigned SPIN_CAP = 1u << 16;

#define WAITV(n) asm volatile("s_waitcnt vmcnt(" #n ")" ::: "memory")
#define WAITL0() asm volatile("s_waitcnt lgkmcnt(0)" ::: "memory")
#define BAR() do { WAITL0(); __builtin_amdgcn_s_barrier(); asm volatile("" ::: "memory"); } while (0)

DI void dma16(unsigned lds, unsigned voff, rsrc_t rs, unsigned soff) {
  asm volatile("s_mov_b32 m0, %0\n\ts_nop 0\n\tbuffer_load_dwordx4 %1, %2, %3 offen lds" :: "s"(lds), "v"(voff), "s"(rs), "s"(soff) : "memory");
}
DI void dma4(unsigned lds, unsigned voff, rsrc_t rs, unsigned soff) {
  asm volatile("s_mov_b32 m0, %0\n\ts_nop 0\n\tbuffer_load_dword %1, %2, %3 offen lds" :: "s"(lds), "v"(voff), "s"(rs), "s"(soff) : "memory");
}
DI void st16_sc1(u32x4 v, unsigned voff, rsrc_t rs, unsigned soff) {
  asm volatile("buffer_store_dwordx4 %0, %1, %2, %3 offen sc1\n\ts_nop 1" :: "v"(v), "v"(voff), "s"(rs), "s"(soff) : "memory");
}
DI void ld16_sc1(u32x4& v, unsigned voff, rsrc_t rs, unsigned soff) {
  asm volatile("buffer_load_dwordx4 %0, %1, %2, %3 offen sc1" : "=v"(v) : "v"(voff), "s"(rs), "s"(soff) : "memory");
}
DI bf16x8 lds_b128(unsigned a) { return *(const LAS bf16x8*)a; }
DI bf16x8 lds_tr(unsigned a0, unsigned a1) {
  bf16x4 lo = __builtin_amdgcn_ds_read_tr16_b64_v4bf16((LAS bf16x4*)a0);
  bf16x4 hi = __builtin_amdgcn_ds_read_tr16_b64_v4bf16((LAS bf16x4*)a1);
  return __builtin_shufflevector(lo, hi, 0, 1, 2, 3, 4, 5, 6, 7);
}
DI f32x16 mfma32(bf16x8 a, bf16x8 b, f32x16 c) { return __builtin_amdgcn_mfma_f32_32x32x16_bf16(a, b, c, 0, 0, 0); }
DI f32x4 mfma16(bf16x8 a, bf16x8 b, f32x4 c) { return __builtin_amdgcn_mfma_f32_16x16x32_bf16(a, b, c, 0, 0, 0); }
template <int O> DI bf16x8 cvt8(const f32x16& c) {
  bf16x8 r;
#pragma unroll
  for (int i = 0; i < 8; ++i) r[i] = (__bf16)c[O + i];
  return r;
}
DI u32x4 nopoison(bf16x8 v) {
  u32x4 u = __builtin_bit_cast(u32x4, v);
#pragma unroll
  for (int i = 0; i < 4; ++i) u[i] = u[i] < 0xFFFFFFFEu ? u[i] : 0xFFFFFFFEu;
  return u;
}
DI unsigned umax4(u32x4 a) { return max(max(a[0], a[1]), max(a[2], a[3])); }

DI void dual_softmax(const f32x16& p, bf16x8 (&f)[4]) {
  float mx = __builtin_fmaxf(p[0], p[1]), mn = __builtin_fminf(p[0], p[1]);
#pragma unroll
  for (int i = 2; i < 16; i += 2) { mx = __builtin_fmaxf(__builtin_fmaxf(mx, p[i]), p[i + 1]); mn = __builtin_fminf(__builtin_fminf(mn, p[i]), p[i + 1]); }
  const float L2E = 1.4426950408889634f;
  const float a = -mx * L2E, b = mn * L2E;
  f32x2 e[8], g[8];
  const f32x2 c2 = {L2E, L2E}, a2 = {a, a}, b2 = {b, b};
#pragma unroll
  for (int i = 0; i < 8; ++i) {
    f32x2 v = {p[2 * i], p[2 * i + 1]};
    f32x2 x = v * c2 + a2, y = b2 - v * c2;
    e[i] = f32x2{__builtin_amdgcn_exp2f(x.x), __builtin_amdgcn_exp2f(x.y)};
    g[i] = f32x2{__builtin_amdgcn_exp2f(y.x), __builtin_amdgcn_exp2f(y.y)};
  }
  f32x2 se = ((e[0] + e[1]) + (e[2] + e[3])) + ((e[4] + e[5]) + (e[6] + e[7]));
  f32x2 sg = ((g[0] + g[1]) + (g[2] + g[3])) + ((g[4] + g[5]) + (g[6] + g[7]));
  const float re = __builtin_amdgcn_rcpf(se.x + se.y), rg = __builtin_amdgcn_rcpf(sg.x + sg.y);
  const f32x2 re2 = {re, re}, rg2 = {rg, rg};
#pragma unroll
  for (int i = 0; i < 8; ++i) {
    f32x2 x = e[i] * re2, y = g[i] * rg2;
    f[i >> 2][2 * (i & 3)] = (__bf16)x.x; f[i >> 2][2 * (i & 3) + 1] = (__bf16)x.y;
    f[2 + (i >> 2)][2 * (i & 3)] = (__bf16)y.x; f[2 + (i >> 2)][2 * (i & 3) + 1] = (__bf16)y.y;
  }
}

#define STAMP(i) do { if ((i) == PROBE) asm volatile("s_memrealtime %0\n\ts_waitcnt lgkmcnt(0)" : "=s"(probe_t) :: "memory"); } while (0)
#define STAMPR(i) do {} while (0)
#define STAMPV(i, x) do {} while (0)

#define SB() __builtin_amdgcn_sched_barrier(0)
DI void cnt_arrive(unsigned a) {
  asm volatile("s_waitcnt lgkmcnt(0)" ::: "memory");
  if ((threadIdx.x & 63) == 0) __hip_atomic_fetch_add((LAS unsigned*)a, 1u, __ATOMIC_RELAXED, __HIP_MEMORY_SCOPE_WORKGROUP);
}
DI void cnt_wait(unsigned a, unsigned n) {
  unsigned v;
  do { asm volatile("ds_read_b32 %0, %1\n\ts_waitcnt lgkmcnt(0)" : "=&v"(v) : "v"(a) : "memory"); } while ((unsigned)__builtin_amdgcn_readfirstlane(v) < n);
}
DI f32x16 zero16() { f32x16 z;
#pragma unroll
  for (int i = 0; i < 16; ++i) z[i] = 0.f;
  return z; }
DI bool has_poison(unsigned m) { return !__all(m != 0xFFFFFFFFu); }

__global__ __launch_bounds__(512, 2) void hedgehog_fused(
    const u16* __restrict__ xA, const u16* __restrict__ wA, const u16* __restrict__ pA, const u16* __restrict__ oA,
    const u16* __restrict__ xB, const u16* __restrict__ wB, const u16* __restrict__ pB, const u16* __restrict__ oB,
    u16* __restrict__ out, char* __restrict__ ws) {
  extern __shared__ __attribute__((aligned(16))) char smem[];
  u64 probe_t = 0;
  STAMPR(60); STAMP(0);
  const unsigned L = (unsigned)(size_t)(LAS char*)smem;
  const int b = blockIdx.x, xcd = b & 7, jj = b >> 3;
  const int br = xcd >> 2, rt = (xcd & 3) * 4 + (jj >> 3), hg = jj & 7;
  const int rot = (hg + 2 * (rt & 3)) & 7;
  const int tid = threadIdx.x, lane = tid & 63;
  const int w = __builtin_amdgcn_readfirstlane(tid >> 6);
  const int u = w & 3;
  const int r32 = lane & 31, h = lane >> 5, blk = (lane >> 4) & 1, q4 = (lane & 15) >> 2, p4 = lane & 3;
  const rsrc_t rsO = __builtin_amdgcn_make_buffer_rsrc((void*)(br ? oB : oA), 0, 512 * 1024, 0x00020000);
  const rsrc_t rsS = __builtin_amdgcn_make_buffer_rsrc((void*)ws, 0, WS_BYTES, 0x00020000);
  const unsigned lid = (unsigned)((br * 16 + rt) * 8 + hg);
  const unsigned vwo = (unsigned)((lane >> 4) * 1024 + (hg & 3) * 256 + (((lane & 15) ^ ((lane >> 4) << 2)) << 4));
  const u32x4 poison = {0xFFFFFFFFu, 0xFFFFFFFFu, 0xFFFFFFFFu, 0xFFFFFFFFu};
  auto issue_wo = [&](int part, int slot, int i0, int i1) {
    const unsigned so = (unsigned)((64 * ((hg + 2 * part + (u >> 1)) & 7) + 32 * (u & 1)) * 1024);
#pragma unroll
    for (int i = i0; i < i1; ++i) dma16(L + slot * SLOT + (u * 8 + i) * 1024, vwo, rsO, so + i * 4096);
  };
  const unsigned fx = (unsigned)(h ^ ((r32 >> 1) & 7));

  if (w >= 4) {
    const rsrc_t rsX = __builtin_amdgcn_make_buffer_rsrc((void*)((br ? xB : xA) + (size_t)rt * 128 * 512), 0, 128 * 1024, 0x00020000);
    const rsrc_t rsW = __builtin_amdgcn_make_buffer_rsrc((void*)(br ? wB : wA), 0, 512 * 3072, 0x00020000);
    const unsigned vxe = (unsigned)((lane >> 3) * 1024 + (((lane & 7) ^ (lane >> 4)) << 4));
    const unsigned vxo = vxe ^ 64u;
    const unsigned vwk = (unsigned)((lane >> 3) * 3072 + hg * 128 + (((lane & 7) ^ (((lane >> 4) & 1) << 2)) << 4));
    const unsigned vwv = vwk ^ 32u;
    auto issue = [&](int v, int i0, int i1) {
      if (v < 8) {
        const unsigned c = (unsigned)((v + rot) & 7), sl = L + (v & 3) * SLOT;
        const unsigned sx = c * 128 + u * 32768, sw = c * (64 * 3072) + u * (16 * 3072);
#pragma unroll
        for (int i = i0; i < i1; ++i) {
          if (i < 2) dma16(sl + 16384 + (2 * u + i) * 1024, vwk, rsW, sw + i * (8 * 3072) + 1024);
          else if (i < 4) dma16(sl + 24576 + (2 * u + i - 2) * 1024, vwv, rsW, sw + (i - 2) * (8 * 3072) + 2048);
          else dma16(sl + (4 * u + i - 4) * 1024, (i & 1) ? vxo : vxe, rsX, sx + (i - 4) * 8192);
        }
      } else if (v < 10) {
#pragma unroll
        for (int i = i0; i < i1; ++i) {
          const unsigned c = (unsigned)((4 * (v - 8) + (i >> 1) + rot) & 7);
          dma16(L + (v - 8) * SLOT + (i >> 1) * 8192 + (2 * u + (i & 1)) * 1024, vwk, rsW, c * (64 * 3072) + (2 * u + (i & 1)) * (8 * 3072));
        }
      } else issue_wo(0, 2, i0, i1);
    };
    asm volatile("s_nop 4" ::: "memory");
    STAMP(50);
    issue(0, 0, 8);
    STAMP(51);
    issue(1, 0, 8);
    bf16x8 xf[32];
    unsigned axk[2][4];
#pragma unroll
    for (int ks = 0; ks < 4; ++ks) { axk[0][ks] = L + (32 * u + r32) * 128 + ((fx ^ (2 * ks)) << 4); axk[1][ks] = axk[0][ks] + 2 * SLOT; asm volatile("" : "+v"(axk[1][ks])); }
#pragma unroll
    for (int s = 0; s < 8; ++s) {
      issue(s + 2, 0, 3);
      WAITV(11);
      BAR();
      if (s == 0) STAMP(2); if (s == 1) STAMP(3); if (s == 2) STAMP(4); if (s == 3) STAMP(5); if (s == 4) STAMP(6); if (s == 5) STAMP(7); if (s == 6) STAMP(8); if (s == 7) STAMP(9);
      issue(s + 2, 3, 4);
#pragma unroll
      for (int ks = 0; ks < 4; ++ks) xf[4 * s + ks] = lds_b128(axk[(s >> 1) & 1][ks] + (s & 1) * SLOT);
      SB();
      issue(s + 2, 4, 8);
    }
    issue(10, 0, 8);
    STAMP(10);
    bf16x8 ap[4];
#pragma unroll
    for (int a = 0; a < 4; ++a) {
      const unsigned base = L + L_PHIW + a * 512 + (8 * h + q4) * 32 + (2 * blk + (p4 >> 1)) * 8;
      const bool on = (p4 & 1) == (a & 1);
      ap[a] = lds_tr(on ? base : L + L_ZERO, on ? base + 128 : L + L_ZERO);
    }
    WAITV(8);
    cnt_arrive(L + L_CNT);
    cnt_wait(L + L_CNT, 8);
    STAMP(11);
    issue_wo(1, 3, 0, 8);
    const unsigned of_own = WS_OF + (unsigned)(((br * 64 + 4 * rt + u) * 32 + 4 * hg) * 1024);
#pragma unroll
    for (int a = 0; a < 4; ++a) st16_sc1(poison, (unsigned)(lane * 16), rsS, of_own + a * 1024);
    STAMP(12);
    f32x16 accQ[2] = {zero16(), zero16()};
    {
      const unsigned aw0 = L + (8 * h + q4) * 128 + (((2 * blk + (p4 & 1)) ^ ((q4 >> 1) << 2)) << 4) + 8 * (p4 >> 1);
      unsigned aw1 = aw0 ^ 64u; asm volatile("" : "+v"(aw1));
      bf16x8 wq[3][2];
#pragma unroll
      for (int s = 0; s < 2; ++s) { wq[s][0] = lds_tr(aw0 + s * 2048, aw0 + s * 2048 + 512); wq[s][1] = lds_tr(aw1 + s * 2048, aw1 + s * 2048 + 512); }
      SB();
#pragma unroll
      for (int s = 0; s < 32; ++s) {
        if (s + 2 < 32) wq[(s + 2) % 3][0] = lds_tr(aw0 + (s + 2) * 2048, aw0 + (s + 2) * 2048 + 512);
        accQ[0] = mfma32(wq[s % 3][0], xf[s], accQ[0]);
        SB();
        if (s + 2 < 32) wq[(s + 2) % 3][1] = lds_tr(aw1 + (s + 2) * 2048, aw1 + (s + 2) * 2048 + 512);
        accQ[1] = mfma32(wq[s % 3][1], xf[s], accQ[1]);
        SB();
      }
    }
    WAITV(0);
    cnt_arrive(L + L_CNT + 4);
    STAMP(13);
    bf16x8 fq[4][2];
#pragma unroll
    for (int hp = 0; hp < 2; ++hp) {
      f32x16 pq = mfma32(ap[2 * hp], cvt8<0>(accQ[hp]), zero16());
      pq = mfma32(ap[2 * hp + 1], cvt8<8>(accQ[hp]), pq);
      bf16x8 f[4];
      dual_softmax(pq, f);
#pragma unroll
      for (int sg = 0; sg < 2; ++sg) {
        u32x4 a = __builtin_bit_cast(u32x4, f[2 * sg]), c = __builtin_bit_cast(u32x4, f[2 * sg + 1]);
#pragma unroll
        for (int i = 0; i < 4; ++i) { auto r = __builtin_amdgcn_permlane32_swap(a[i], c[i], false, false); a[i] = r[0]; c[i] = r[1]; }
        fq[2 * hp][sg] = __builtin_bit_cast(bf16x8, a);
        fq[2 * hp + 1][sg] = __builtin_bit_cast(bf16x8, c);
      }
    }
#pragma unroll
    for (int a = 0; a < 4; ++a) asm volatile("" : "+v"(fq[a][0]), "+v"(fq[a][1]));
    const u64 tag = *(const LAS u64*)(L + L_TAG);
    u32x2 fl;
    const unsigned flo = WS_FLAG + (unsigned)((br * 16 + rt) * 8 + (lane & 7)) * 128 + 64;
    asm volatile("buffer_load_dwordx2 %0, %1, %2, 0 offen sc1" : "=v"(fl) : "v"(flo), "s"(rsS) : "memory");
    unsigned kb = L + L_SCR + h * 320 + (r32 < 16 ? r32 : 16 + (r32 & 3)) * 16; asm volatile("" : "+v"(kb));
    unsigned aown = L + L_SCR + 2048 + (u & 1) * 1024 + lane * 16; asm volatile("" : "+v"(aown));
    unsigned aow = L + 2 * SLOT + u * 2048 + lane * 16; asm volatile("" : "+v"(aow));
    STAMP(14);
    BAR();
    STAMP(26);
    {
      bf16x8 ka[4][2];
#pragma unroll
      for (int a = 0; a < 4; ++a) { ka[a][0] = lds_b128(kb + a * SCR); ka[a][1] = lds_b128(kb + a * SCR + 640); }
      f32x16 n[4];
#pragma unroll
      for (int a = 0; a < 4; ++a) n[a] = mfma32(ka[a][0], fq[a][0], zero16());
#pragma unroll
      for (int a = 0; a < 4; ++a) n[a] = mfma32(ka[a][1], fq[a][1], n[a]);
      const bool own = (u >> 1) == (hg >> 2);
#pragma unroll
      for (int a = 0; a < 4; ++a) {
        const float rd = __builtin_amdgcn_rcpf(((n[a][8] + n[a][9]) + (n[a][10] + n[a][11])) + 1e-6f);
        const f32x2 r2 = {rd, rd};
        bf16x8 o;
#pragma unroll
        for (int i = 0; i < 4; ++i) { f32x2 v = f32x2{n[a][2 * i], n[a][2 * i + 1]} * r2; o[2 * i] = (__bf16)v.x; o[2 * i + 1] = (__bf16)v.y; }
        const u32x4 ou = nopoison(o);
        st16_sc1(ou, (unsigned)(lane * 16), rsS, of_own + a * 1024);
        if (own) *(LAS u32x4*)(aown + a * SCR) = ou;
      }
    }
    STAMP(27);
    BAR();
    STAMP(28);
    WAITV(0);
    asm volatile("" : "+v"(fl));
    {
      unsigned spins = 0;
      while (!__all((((u64)fl.y << 32) | fl.x) == tag) && ++spins < SPIN_CAP)
        asm volatile("buffer_load_dwordx2 %0, %1, %2, 0 offen sc1\n\ts_waitcnt vmcnt(0)" : "=&v"(fl) : "v"(flo), "s"(rsS) : "memory");
    }
    STAMP(29);
    const unsigned of_src = WS_OF + (unsigned)(((br * 64 + 4 * rt + 2 * (hg >> 2)) * 32 + u) * 1024);
    u32x4 g[8][2];
    auto gload = [&](int st) {
      const unsigned so = of_src + (unsigned)(((hg + st) & 7) * 4096);
      ld16_sc1(g[st][0], (unsigned)(lane * 16), rsS, so);
      ld16_sc1(g[st][1], (unsigned)(lane * 16), rsS, so + 32768);
    };
    gload(1); gload(2); gload(3); gload(4);
    STAMP(30);
    unsigned spins2 = 0;
#pragma unroll
    for (int st = 1; st < 8; ++st) {
      if (st <= 4) WAITV(6); else if (st == 5) WAITV(4); else if (st == 6) WAITV(2); else WAITV(0);
      if (st == 1) STAMP(31);
      for (;;) {
        asm volatile("" : "+v"(g[st][0]), "+v"(g[st][1]));
        if (!has_poison(max(umax4(g[st][0]), umax4(g[st][1]))) || ++spins2 >= SPIN_CAP) break;
        gload(st);
        WAITV(0);
      }
      *(LAS u32x4*)(aow + (st - 1) * 8192) = g[st][0];
      *(LAS u32x4*)(aow + (st - 1) * 8192 + 1024) = g[st][1];
      if (st + 4 < 8) gload(st + 4);
      BAR();
      if (st == 1) STAMP(32); if (st == 2) STAMP(33); if (st == 3) STAMP(34); if (st == 4) STAMP(35); if (st == 5) STAMP(36); if (st == 6) STAMP(37); if (st == 7) STAMP(38);
    }
    STAMPV(63, spins2);
    if (rt == 0 && hg == 0 && tid == 256) __hip_atomic_store((GAS u64*)(ws + WS_EPOCH + br * 128), tag, __ATOMIC_RELAXED, __HIP_MEMORY_SCOPE_AGENT);
  } else {
    const unsigned kvp_own = WS_KVP + (unsigned)((br * 8 + hg) * 16 + rt) * KVP_STR;
    {
      const rsrc_t rsP = __builtin_amdgcn_make_buffer_rsrc((void*)(br ? pB : pA), 0, 32 * 512, 0x00020000);
      asm volatile("s_nop 4" ::: "memory");
      if (u < 2) dma16(L + L_PHIW + u * 1024, (unsigned)(lane * 16), rsP, (unsigned)(hg * 2048 + u * 1024));
      st16_sc1(poison, (unsigned)(lane * 16), rsS, kvp_own + u * 1024);
      st16_sc1(poison, lane < 4 ? (unsigned)(lane * 16) : 0x80000000u, rsS, kvp_own + 4096 + u * 64);
      if (u >= 2) *(LAS u32x4*)(L + L_ONES + (u - 2) * 1024 + lane * 16) = u32x4{0x3F803F80u, 0x3F803F80u, 0x3F803F80u, 0x3F803F80u};
      if (u == 0 && lane < 2) *(LAS u32x4*)(L + L_ZERO + lane * 16) = u32x4{0, 0, 0, 0};
    }
    u64 tag;
    asm volatile("s_load_dwordx2 %0, %1, 0x0\n\ts_waitcnt lgkmcnt(0)" : "=&s"(tag) : "s"(ws + WS_EPOCH + br * 128) : "memory");
    tag += 1;
    if (tid == 0) *(LAS u64*)(L + L_TAG) = tag;
    f32x16 acc[4] = {zero16(), zero16(), zero16(), zero16()};
    unsigned axk[2][4];
#pragma unroll
    for (int ks = 0; ks < 4; ++ks) { axk[0][ks] = L + r32 * 128 + ((fx ^ (2 * ks)) << 4); axk[1][ks] = axk[0][ks] + 2 * SLOT; asm volatile("" : "+v"(axk[0][ks]), "+v"(axk[1][ks])); }
    unsigned awb[2];
    awb[0] = L + 16384 + blk * 8192 + (8 * h + q4) * 128 + (((2 * u + (p4 & 1)) ^ ((q4 >> 1) << 2) ^ (blk << 1)) << 4) + 8 * (p4 >> 1);
    awb[1] = awb[0] + 2 * SLOT; asm volatile("" : "+v"(awb[0]), "+v"(awb[1]));
    bf16x8 fa[3], fb[3][4];
    STAMP(1);
#pragma unroll
    for (int s = 0; s < 8; ++s) {
      if (s == 2) WAITV(0);
      BAR();
      if (s == 0) STAMP(2); if (s == 1) STAMP(3); if (s == 2) STAMP(4); if (s == 3) STAMP(5); if (s == 4) STAMP(6); if (s == 5) STAMP(7); if (s == 6) STAMP(8); if (s == 7) STAMP(9);
      if (s == 2 && tid == 0) __hip_atomic_store((GAS u64*)(ws + WS_FLAG + lid * 128), tag, __ATOMIC_RELAXED, __HIP_MEMORY_SCOPE_AGENT);
      const unsigned sl = (s & 1) * SLOT;
#define RD_A(ks) fa[(4 * s + (ks)) % 3] = lds_tr(awb[(s >> 1) & 1] + sl + (ks) * 2048, awb[(s >> 1) & 1] + sl + (ks) * 2048 + 512)
#define RD_B(ks, t) fb[(4 * s + (ks)) % 3][t] = lds_b128(axk[(s >> 1) & 1][ks] + sl + (t) * 4096)
#define MM(n, t) acc[t] = mfma32(fa[(n) % 3], fb[(n) % 3][t], acc[t])
      SB();
      if (s > 0) {
        MM(4 * s - 1, 0); SB(); RD_A(0); RD_B(0, 0); SB();
        MM(4 * s - 1, 1); SB(); RD_B(0, 1); RD_B(0, 2); RD_B(0, 3); SB();
        MM(4 * s - 1, 2); SB(); RD_A(1); RD_B(1, 0); SB();
        MM(4 * s - 1, 3); SB(); RD_B(1, 1); RD_B(1, 2); RD_B(1, 3); SB();
      } else {
        RD_A(0); RD_B(0, 0); RD_B(0, 1); RD_B(0, 2); RD_B(0, 3); RD_A(1); RD_B(1, 0); RD_B(1, 1); RD_B(1, 2); RD_B(1, 3); SB();
      }
      MM(4 * s, 0); SB(); RD_A(2); SB();
      MM(4 * s, 1); SB(); RD_B(2, 0); RD_B(2, 1); SB();
      MM(4 * s, 2); SB(); RD_B(2, 2); RD_B(2, 3); SB();
      MM(4 * s, 3); SB();
      MM(4 * s + 1, 0); SB(); RD_A(3); SB();
      MM(4 * s + 1, 1); SB(); RD_B(3, 0); RD_B(3, 1); SB();
      MM(4 * s + 1, 2); SB(); RD_B(3, 2); RD_B(3, 3); SB();
      MM(4 * s + 1, 3); SB();
      MM(4 * s + 2, 0); MM(4 * s + 2, 1); MM(4 * s + 2, 2); MM(4 * s + 2, 3); SB();
    }
    cnt_arrive(L + L_CNT);
    bf16x8 ap[2];
#pragma unroll
    for (int s = 0; s < 2; ++s) {
      const unsigned base = L + L_PHIW + u * 512 + (8 * h + q4) * 32 + (2 * blk + (p4 >> 1)) * 8;
      const bool on = (p4 & 1) == s;
      ap[s] = lds_tr(on ? base : L + L_ZERO, on ? base + 128 : L + L_ZERO);
    }
    u32x2 fl;
    const unsigned flo = WS_FLAG + (unsigned)((br * 16 + (lane & 15)) * 8 + hg) * 128;
    asm volatile("buffer_load_dwordx2 %0, %1, %2, 0 offen sc1" : "=v"(fl) : "v"(flo), "s"(rsS) : "memory");
    MM(31, 0); MM(31, 1); MM(31, 2); MM(31, 3);
    STAMP(10);
    __builtin_amdgcn_s_setprio(2);
    const unsigned scr = L + L_SCR + u * SCR;
    f32x16 y = zero16();
    {
      const unsigned tq = (unsigned)lane;
      const unsigned pw = scr + tq * 64, sw = (tq >> 1) & 3;
      const unsigned vw0 = scr + 4096 + r32 * 32 + ((h ^ ((r32 >> 2) & 1)) << 4);
      unsigned aa = scr + (8 * h + q4) * 64 + 8 * (p4 >> 1);
      const unsigned aa0 = aa + (((2 * blk + (p4 & 1)) ^ (q4 >> 1)) << 4), aa1 = aa + 256 + (((2 * blk + (p4 & 1)) ^ (2 + (q4 >> 1))) << 4);
      const unsigned ab = scr + 4096 + (8 * h + q4) * 32 + 8 * (p4 & 1);
      const unsigned ab0 = blk ? L + L_ONES + 128 : ab + ((p4 >> 1) << 4), ab1 = blk ? L + L_ONES + 256 : ab + 128 + (((p4 >> 1) ^ 1) << 4);
      f32x16 pk[2];
#pragma unroll
      for (int gm = 0; gm < 2; ++gm) {
        pk[gm] = mfma32(ap[0], cvt8<0>(acc[2 * gm]), zero16());
        pk[gm] = mfma32(ap[1], cvt8<0>(acc[2 * gm + 1]), pk[gm]);
      }
#pragma unroll
      for (int gm = 0; gm < 2; ++gm) {
        bf16x8 f[4];
        dual_softmax(pk[gm], f);
        *(LAS bf16x8*)(vw0) = cvt8<8>(acc[2 * gm]);
        *(LAS bf16x8*)(vw0 + 1024) = cvt8<8>(acc[2 * gm + 1]);
#pragma unroll
        for (int c = 0; c < 4; ++c) *(LAS bf16x8*)(pw + ((c ^ sw) << 4)) = f[c];
        asm volatile("" ::: "memory");
        bf16x8 ra[4], rb[4];
#pragma unroll
        for (int k2 = 0; k2 < 4; ++k2) { ra[k2] = lds_tr(aa0 + k2 * 1024, aa1 + k2 * 1024); rb[k2] = lds_tr(ab0 + k2 * 512, ab1 + k2 * 512); }
        asm volatile("" ::: "memory");
#pragma unroll
        for (int k2 = 0; k2 < 4; ++k2) y = mfma32(ra[k2], rb[k2], y);
        if (gm == 0) STAMP(15);
      }
    }
    {
      const unsigned so = blk == 0 ? (unsigned)((u * 4 + h) * 256 + (lane & 15) * 16) : ((lane & 15) == 0 ? (unsigned)(4096 + (u * 4 + h) * 16) : 0x80000000u);
      const unsigned ss = blk == 0 ? 512u : 32u;
      st16_sc1(nopoison(cvt8<0>(y)), so, rsS, kvp_own);
      st16_sc1(nopoison(cvt8<8>(y)), so + ss, rsS, kvp_own);
    }
    __builtin_amdgcn_s_setprio(0);
    STAMP(16);
    bf16x8 sel[2];
#pragma unroll
    for (int v = 0; v < 2; ++v)
#pragma unroll
      for (int j = 0; j < 8; ++j) sel[v][j] = ((lane & 15) == 8 * v + j) ? (__bf16)1.0f : (__bf16)0.0f;
    asm volatile("" : "+v"(sel[0]), "+v"(sel[1]));
    const unsigned kvp_grp = WS_KVP + (unsigned)((br * 8 + hg) * 16) * KVP_STR;
    const unsigned gv = (unsigned)((lane >> 4) * KVP_STR + (lane & 15) * 16);
    const unsigned gzv = (unsigned)((lane >> 2) * KVP_STR + (lane & 3) * 16);
    unsigned kw = scr + (lane >> 5) * 320 + (lane & 15) * 16 + ((lane >> 4) & 1) * 8; asm volatile("" : "+v"(kw));
    unsigned zw = scr + (lane & 3) * 320 + 256 + ((lane >> 2) & 3) * 16 + ((lane >> 4) & 1) * 8; asm volatile("" : "+v"(zw));
    WAITV(0);
    asm volatile("" : "+v"(fl));
    {
      unsigned spins = 0;
      while (!__all((((u64)fl.y << 32) | fl.x) == tag) && ++spins < SPIN_CAP)
        asm volatile("buffer_load_dwordx2 %0, %1, %2, 0 offen sc1\n\ts_waitcnt vmcnt(0)" : "=&v"(fl) : "v"(flo), "s"(rsS) : "memory");
    }
    STAMP(17);
    u32x4 g[16], gz;
    auto gather_kv = [&]() {
#pragma unroll
      for (int i = 0; i < 16; ++i) ld16_sc1(g[i], gv, rsS, kvp_grp + (i & 3) * 4 * KVP_STR + (4 * u + (i >> 2)) * 256);
      ld16_sc1(gz, gzv, rsS, kvp_grp + 4096 + u * 64);
    };
    gather_kv();
    STAMP(18);
    cnt_wait(L + L_CNT + 4, 4);
    if (tid == 0) __hip_atomic_store((GAS u64*)(ws + WS_FLAG + lid * 128 + 64), tag, __ATOMIC_RELAXED, __HIP_MEMORY_SCOPE_AGENT);
    STAMP(19);
    issue_wo(2, 0, 0, 8);
    STAMP(20);
    WAITV(8);
    STAMP(21);
    {
      unsigned spins = 0;
      f32x4 d0, d1, dz;
      for (;;) {
#pragma unroll
        for (int i = 0; i < 16; ++i) asm volatile("" : "+v"(g[i]));
        asm volatile("" : "+v"(gz));
        d0 = f32x4{0.f, 0.f, 0.f, 0.f}; d1 = d0;
#pragma unroll
        for (int i = 0; i < 8; ++i) { d0 = mfma16(sel[i >> 2], __builtin_bit_cast(bf16x8, g[i]), d0); d1 = mfma16(sel[i >> 2], __builtin_bit_cast(bf16x8, g[8 + i]), d1); }
        dz = mfma16(sel[0], __builtin_bit_cast(bf16x8, gz), f32x4{0.f, 0.f, 0.f, 0.f});
        const float chk = (d0[0] + d1[0]) + dz[0];
        if (__all(chk == chk)) break;
        unsigned m = umax4(gz);
#pragma unroll
        for (int i = 0; i < 16; ++i) m = max(m, umax4(g[i]));
        if (!has_poison(m) || spins >= SPIN_CAP) break;
        do {
          ld16_sc1(gz, gzv, rsS, kvp_grp + 4096 + u * 64);
          WAITV(0);
          asm volatile("" : "+v"(gz));
        } while (++spins < SPIN_CAP && has_poison(umax4(gz)));
        gather_kv();
        WAITV(0);
      }
      STAMPV(62, spins);
      *(LAS bf16x4*)(kw) = bf16x4{(__bf16)d0[0], (__bf16)d0[1], (__bf16)d0[2], (__bf16)d0[3]};
      *(LAS bf16x4*)(kw + 640) = bf16x4{(__bf16)d1[0], (__bf16)d1[1], (__bf16)d1[2], (__bf16)d1[3]};
      if (lane < 32) *(LAS bf16x4*)(zw) = bf16x4{(__bf16)dz[0], (__bf16)dz[1], (__bf16)dz[2], (__bf16)dz[3]};
    }
    STAMP(25);
    BAR();
    STAMP(26);
    issue_wo(3, 1, 0, 8);
    bf16x8 wf[32];
    const unsigned awo = L + (4 * h + q4) * 256 + (((4 * u + 2 * (p4 & 1) + blk) ^ (q4 << 2)) << 4) + 8 * (p4 >> 1);
    unsigned awo2 = awo + 2 * SLOT; asm volatile("" : "+v"(awo2));
#pragma unroll
    for (int hd = 0; hd < 16; ++hd) wf[hd] = lds_tr(awo2 + hd * 4096, awo2 + hd * 4096 + 2048);
    unsigned aor0 = L + L_SCR + 2048 + lane * 16; asm volatile("" : "+v"(aor0));
    unsigned aor = L + 2 * SLOT + lane * 16; asm volatile("" : "+v"(aor));
    STAMP(27);
    BAR();
    STAMP(28);
    f32x16 c2[2] = {zero16(), zero16()};
    bf16x8 ob[2][8];
#pragma unroll
    for (int i = 0; i < 8; ++i) ob[0][i] = lds_b128(aor0 + (i >> 1) * SCR + (i & 1) * 1024);
#pragma unroll
    for (int i = 0; i < 8; ++i) c2[i & 1] = mfma32(wf[i >> 1], ob[0][i], c2[i & 1]);
    SB();
    WAITV(0);
    cnt_arrive(L + L_CNT + 8);
    cnt_wait(L + L_CNT + 8, 4);
#pragma unroll
    for (int hd = 16; hd < 32; ++hd) wf[hd] = lds_tr(awo + (hd - 16) * 4096, awo + (hd - 16) * 4096 + 2048);
    STAMP(30);
#pragma unroll
    for (int st = 1; st < 8; ++st) {
      BAR();
      if (st == 1) STAMP(32); if (st == 2) STAMP(33); if (st == 3) STAMP(34); if (st == 4) STAMP(35); if (st == 5) STAMP(36); if (st == 6) STAMP(37); if (st == 7) STAMP(38);
      SB();
#pragma unroll
      for (int i = 0; i < 8; ++i) {
        if (st > 1) { c2[i & 1] = mfma32(wf[4 * (st - 1) + (i >> 1)], ob[(st - 1) & 1][i], c2[i & 1]); SB(); }
        if (i < 4) { ob[st & 1][2 * i] = lds_b128(aor + (st - 1) * 8192 + (2 * i) * 1024); ob[st & 1][2 * i + 1] = lds_b128(aor + (st - 1) * 8192 + (2 * i + 1) * 1024); SB(); }
      }
    }
#pragma unroll
    for (int i = 0; i < 8; ++i) c2[i & 1] = mfma32(wf[28 + (i >> 1)], ob[1][i], c2[i & 1]);
    STAMP(39);
    {
#pragma unroll
      for (int t = 0; t < 2; ++t) {
        *(LAS bf16x8*)(scr + t * 2048 + r32 * 64 + (((2 * h) ^ ((r32 >> 1) & 3)) << 4)) = cvt8<0>(c2[t]);
        *(LAS bf16x8*)(scr + t * 2048 + r32 * 64 + (((2 * h + 1) ^ ((r32 >> 1) & 3)) << 4)) = cvt8<8>(c2[t]);
      }
      asm volatile("" ::: "memory");
      u16* op = out + (size_t)(rt * 128 + (hg >> 2) * 64 + (lane >> 2)) * 1024 + br * 512 + (hg & 3) * 128 + u * 32 + (lane & 3) * 8;
      const unsigned rd = scr + (lane >> 2) * 64 + (((lane & 3) ^ ((lane >> 3) & 3)) << 4);
#pragma unroll
      for (int i = 0; i < 4; ++i) {
        bf16x8 v = lds_b128(rd + i * 1024);
        asm volatile("global_store_dwordx4 %0, %1, off sc1\n\ts_nop 1" :: "v"(op + (size_t)i * 16 * 1024), "v"(v) : "memory");
      }
    }
  }
  STAMP(40);
  WAITV(0);
  STAMP(41);
  if (probe_t) { u64 now; do { __builtin_amdgcn_s_sleep(4); asm volatile("s_memrealtime %0\n\ts_waitcnt lgkmcnt(0)" : "=s"(now) :: "memory"); } while (now < probe_t + 4000); }
}

extern "C" void kernel_launch(void* const* d_in, const int* in_sizes, int n_in, void* d_out, int out_size, void* d_ws, size_t ws_size, hipStream_t stream) {
  (void)hipFuncSetAttribute((const void*)hedgehog_fused, hipFuncAttributeMaxDynamicSharedMemorySize, L_BYTES);
  hedgehog_fused<<<256, 512, L_BYTES, stream>>>((const u16*)d_in[0], (const u16*)d_in[1], (const u16*)d_in[2], (const u16*)d_in[3],
                                                (const u16*)d_in[4], (const u16*)d_in[5], (const u16*)d_in[6], (const u16*)d_in[7],
                                                (u16*)d_out, (char*)d_ws);
}
```

```cpp
#include <hip/hip_runtime.h>
#include <hip/hip_bf16.h>
#include <type_traits>
#pragma clang diagnostic ignored "-Wint-to-pointer-cast"

typedef unsigned short u16;
typedef unsigned long long u64;
typedef __bf16 bf16x8 __attribute__((ext_vector_type(8)));
typedef __bf16 bf16x4 __attribute__((ext_vector_type(4)));
typedef __bf16 bf16x2 __attribute__((ext_vector_type(2)));
typedef float f32x16 __attribute__((ext_vector_type(16)));
typedef float f32x4 __attribute__((ext_vector_type(4)));
typedef float f32x2 __attribute__((ext_vector_type(2)));
typedef unsigned u32x4 __attribute__((ext_vector_type(4)));
typedef unsigned u32x2 __attribute__((ext_vector_type(2)));
#define LAS __attribute__((address_space(3)))
#define GAS __attribute__((address_space(1)))
#define DI __device__ __forceinline__
typedef __amdgpu_buffer_rsrc_t rsrc_t;

#define OUTMODE 1
#define ABL 0
#define PSLEEP 4
#define STAMPS 0

constexpr unsigned WS_EPOCH = 0;
constexpr unsigned WS_FLAG  = 4096;
constexpr unsigned WS_KVP   = 131072;
constexpr unsigned KVP_STR  = 4608;
constexpr unsigned WS_OF    = 2u << 20;
constexpr unsigned WS_BYTES = 6u << 20;
constexpr unsigned WS_STAMP = 8u << 20;
constexpr unsigned SLOT   = 32768;
constexpr unsigned L_SCR  = 131072;
constexpr unsigned SCR    = 6144;
constexpr unsigned L_PHIW = L_SCR + 4 * SCR;
constexpr unsigned L_ZERO = L_PHIW + 2048;
constexpr unsigned L_CNT  = L_ZERO + 16;
constexpr unsigned L_TAG  = L_CNT + 32;
constexpr unsigned L_BYTES = 163840;
constexpr unsigned SPIN_CAP = 1u << 16;

#define WAITV(n) asm volatile("s_waitcnt vmcnt(" #n ")" ::: "memory")
#define WAITL0() asm volatile("s_waitcnt lgkmcnt(0)" ::: "memory")
#define BAR() do { WAITL0(); __builtin_amdgcn_s_barrier(); asm volatile("" ::: "memory"); } while (0)

DI void dma16(unsigned lds, unsigned voff, rsrc_t rs, unsigned soff) {
  asm volatile("s_mov_b32 m0, %0\n\ts_nop 0\n\tbuffer_load_dwordx4 %1, %2, %3 offen lds" :: "s"(lds), "v"(voff), "s"(rs), "s"(soff) : "memory");
}
template <int LI, int SI> DI void dmai(unsigned lds, unsigned voff, rsrc_t rs, unsigned soff) {
  unsigned t;
  asm volatile("s_add_u32 m0, %1, %5\n\ts_add_u32 %0, %4, %6\n\tbuffer_load_dwordx4 %2, %3, %0 offen lds"
               : "=&s"(t) : "s"(lds), "v"(voff), "s"(rs), "s"(soff), "i"(LI), "i"(SI) : "memory", "scc");
}
template <int I, int N, class F> DI void sfor(F&& f) { if constexpr (I < N) { f(std::integral_constant<int, I>{}); sfor<I + 1, N>(f); } }
DI void st16_sc1(u32x4 v, unsigned voff, rsrc_t rs, unsigned soff) {
  asm volatile("buffer_store_dwordx4 %0, %1, %2, %3 offen sc1\n\ts_nop 1" :: "v"(v), "v"(voff), "s"(rs), "s"(soff) : "memory");
}
DI void ld16_sc1(u32x4& v, unsigned voff, rsrc_t rs, unsigned soff) {
  asm volatile("buffer_load_dwordx4 %0, %1, %2, %3 offen sc1" : "=v"(v) : "v"(voff), "s"(rs), "s"(soff) : "memory");
}
DI bf16x8 lds_b128(unsigned a) { return *(const LAS bf16x8*)a; }
DI bf16x8 lds_tr(unsigned a0, unsigned a1) {
  bf16x4 lo = __builtin_amdgcn_ds_read_tr16_b64_v4bf16((LAS bf16x4*)a0);
  bf16x4 hi = __builtin_amdgcn_ds_read_tr16_b64_v4bf16((LAS bf16x4*)a1);
  return __builtin_shufflevector(lo, hi, 0, 1, 2, 3, 4, 5, 6, 7);
}
DI f32x16 mfma32(bf16x8 a, bf16x8 b, f32x16 c) { return __builtin_amdgcn_mfma_f32_32x32x16_bf16(a, b, c, 0, 0, 0); }
DI f32x4 mfma16(bf16x8 a, bf16x8 b, f32x4 c) { return __builtin_amdgcn_mfma_f32_16x16x32_bf16(a, b, c, 0, 0, 0); }
template <int O> DI bf16x8 cvt8(const f32x16& c) {
  bf16x8 r;
#pragma unroll
  for (int i = 0; i < 8; ++i) r[i] = (__bf16)c[O + i];
  return r;
}
DI u32x4 nopoison(bf16x8 v) {
  u32x4 u = __builtin_bit_cast(u32x4, v);
#pragma unroll
  for (int i = 0; i < 4; ++i) u[i] = u[i] < 0xFFFFFFFEu ? u[i] : 0xFFFFFFFEu;
  return u;
}
DI unsigned umax4(u32x4 a) { return max(max(a[0], a[1]), max(a[2], a[3])); }

DI void dual_softmax(const f32x16& p, bf16x8 (&f)[4]) {
  float mx = __builtin_fmaxf(p[0], p[1]), mn = __builtin_fminf(p[0], p[1]);
#pragma unroll
  for (int i = 2; i < 16; i += 2) { mx = __builtin_fmaxf(__builtin_fmaxf(mx, p[i]), p[i + 1]); mn = __builtin_fminf(__builtin_fminf(mn, p[i]), p[i + 1]); }
  const float L2E = 1.4426950408889634f;
  const float a = -mx * L2E, b = mn * L2E;
  f32x2 e[8], g[8];
  const f32x2 c2 = {L2E, L2E}, a2 = {a, a}, b2 = {b, b};
#pragma unroll
  for (int i = 0; i < 8; ++i) {
    f32x2 v = {p[2 * i], p[2 * i + 1]};
    f32x2 x = v * c2 + a2, y = b2 - v * c2;
    e[i] = f32x2{__builtin_amdgcn_exp2f(x.x), __builtin_amdgcn_exp2f(x.y)};
    g[i] = f32x2{__builtin_amdgcn_exp2f(y.x), __builtin_amdgcn_exp2f(y.y)};
  }
  f32x2 se = ((e[0] + e[1]) + (e[2] + e[3])) + ((e[4] + e[5]) + (e[6] + e[7]));
  f32x2 sg = ((g[0] + g[1]) + (g[2] + g[3])) + ((g[4] + g[5]) + (g[6] + g[7]));
  const float re = __builtin_amdgcn_rcpf(se.x + se.y), rg = __builtin_amdgcn_rcpf(sg.x + sg.y);
  const f32x2 re2 = {re, re}, rg2 = {rg, rg};
#pragma unroll
  for (int i = 0; i < 8; ++i) {
    f32x2 x = e[i] * re2, y = g[i] * rg2;
    f[i >> 2][2 * (i & 3)] = (__bf16)x.x; f[i >> 2][2 * (i & 3) + 1] = (__bf16)x.y;
    f[2 + (i >> 2)][2 * (i & 3)] = (__bf16)y.x; f[2 + (i >> 2)][2 * (i & 3) + 1] = (__bf16)y.y;
  }
}

#define STAMP(i) do {} while (0)
#define STAMPR(i) do {} while (0)
#define STAMPV(i, x) do {} while (0)

#define SB() __builtin_amdgcn_sched_barrier(0)
template <int C> DI void cnt_arrive(unsigned base, unsigned one) {
  asm volatile("s_mov_b64 exec, 1\n\tds_add_u32 %0, %1 offset:%2\n\ts_mov_b64 exec, -1" :: "v"(base), "v"(one), "i"(4 * C) : "memory");
}
template <int C> DI void cnt_wait(unsigned base, unsigned waves) {
  unsigned v;
  for (;;) {
    asm volatile("ds_read_b32 %0, %1 offset:%2\n\ts_waitcnt lgkmcnt(0)" : "=&v"(v) : "v"(base), "i"(4 * C) : "memory");
    if ((unsigned)__builtin_amdgcn_readfirstlane(v) >= waves) break;
    __builtin_amdgcn_s_sleep(1);
  }
}
DI void flag_store(u64 tag, unsigned voff, rsrc_t rs, unsigned soff) {
  asm volatile("buffer_store_dwordx2 %0, %1, %2, %3 offen sc1" :: "v"(tag), "v"(voff), "s"(rs), "s"(soff) : "memory");
}
DI void flag_check(u64& fl, u64 tag, unsigned flo, rsrc_t rs) {
  asm volatile("" : "+v"(fl));
  unsigned spins = 0;
  while (!__all(fl == tag) && ++spins < SPIN_CAP)
    asm volatile("buffer_load_dwordx2 %0, %1, %2, 0 offen sc1\n\ts_waitcnt vmcnt(0)" : "=&v"(fl) : "v"(flo), "s"(rs) : "memory");
}
#define RAWBAR() do { asm volatile("" ::: "memory"); __builtin_amdgcn_s_barrier(); asm volatile("" ::: "memory"); } while (0)
DI f32x16 zero16() { f32x16 z;
#pragma unroll
  for (int i = 0; i < 16; ++i) z[i] = 0.f;
  return z; }
DI bool has_poison(unsigned m) { return !__all(m != 0xFFFFFFFFu); }

__global__ __launch_bounds__(512, 2) void hedgehog_fused(
    const u16* __restrict__ xA, const u16* __restrict__ wA, const u16* __restrict__ pA, const u16* __restrict__ oA,
    const u16* __restrict__ xB, const u16* __restrict__ wB, const u16* __restrict__ pB, const u16* __restrict__ oB,
    u16* __restrict__ out, char* __restrict__ ws) {
  extern __shared__ __attribute__((aligned(16))) char smem[];
  STAMPR(60); STAMP(0);
  const unsigned L = (unsigned)(size_t)(LAS char*)smem;
  const int b = blockIdx.x, xcd = b & 7, jj = b >> 3;
  const int br = xcd >> 2, rt = (xcd & 3) * 4 + (jj >> 3), hg = jj & 7;
  const int rot = (hg + 2 * (rt & 3)) & 7;
  const int tid = threadIdx.x, lane = tid & 63;
  const int w = __builtin_amdgcn_readfirstlane(tid >> 6);
  const int u = w & 3;
  const int r32 = lane & 31, h = lane >> 5, blk = (lane >> 4) & 1, q4 = (lane & 15) >> 2, p4 = lane & 3;
  const unsigned lid = (unsigned)((br * 16 + rt) * 8 + hg);
  const unsigned kvp_own = WS_KVP + (unsigned)((br * 8 + hg) * 16 + rt) * KVP_STR;
  const unsigned fx = (unsigned)(h ^ ((r32 >> 1) & 7));
#define MAKE_WO() \
  const rsrc_t rsO = __builtin_amdgcn_make_buffer_rsrc((void*)(br ? oB : oA), 0, 512 * 1024, 0x00020000); \
  const rsrc_t rsS = __builtin_amdgcn_make_buffer_rsrc((void*)ws, 0, WS_BYTES, 0x00020000); \
  const unsigned vwo = (unsigned)((lane >> 4) * 1024 + (hg & 3) * 256 + (((lane & 15) ^ ((lane >> 4) << 2)) << 4)); \
  unsigned cb = L + L_CNT, one = 1; asm volatile("" : "+v"(cb), "+v"(one)); \
  const unsigned lwo = L + 2 * SLOT + u * 8192; \
  auto issue_wo = [&](auto P) { \
    constexpr int part = decltype(P)::value; \
    const unsigned so = (unsigned)((64 * ((hg + 2 * part + (u >> 1)) & 7) + 32 * (u & 1)) * 1024); \
    sfor<0, 8>([&](auto I) { constexpr int i = decltype(I)::value; dmai<(part & 1) * SLOT + i * 1024, i * 4096>(lwo, vwo, rsO, so); }); \
  };
#define IC(n) std::integral_constant<int, n>{}

  if (w >= 4) {
    const rsrc_t rsX = __builtin_amdgcn_make_buffer_rsrc((void*)((br ? xB : xA) + (size_t)rt * 128 * 512), 0, 128 * 1024, 0x00020000);
    const rsrc_t rsW = __builtin_amdgcn_make_buffer_rsrc((void*)(br ? wB : wA), 0, 512 * 3072, 0x00020000);
    const unsigned vxe = (unsigned)((lane >> 3) * 1024 + (((lane & 7) ^ (lane >> 4)) << 4));
    const unsigned vxo = vxe ^ 64u;
    const unsigned vwk = (unsigned)((lane >> 3) * 3072 + hg * 128 + (((lane & 7) ^ (((lane >> 4) & 1) << 2)) << 4));
    const unsigned vwv = vwk ^ 32u;
    const unsigned lx = L + u * 4096, lw = L + u * 2048;
    auto issue = [&](auto V, auto I0, auto I1) {
      constexpr int v = decltype(V)::value;
      if constexpr (v < 8) {
        const unsigned c = (unsigned)((v + rot) & 7);
        const unsigned sx = c * 128 + u * 32768, sw = c * (64 * 3072) + u * (16 * 3072);
        sfor<decltype(I0)::value, decltype(I1)::value>([&](auto I) {
          constexpr int i = decltype(I)::value;
          if constexpr (i < 4) dmai<(v & 3) * SLOT + i * 1024, i * 8192>(lx, (i & 1) ? vxo : vxe, rsX, sx);
          else if constexpr (i < 6) dmai<(v & 3) * SLOT + 16384 + (i - 4) * 1024, (i - 4) * (8 * 3072) + 1024>(lw, vwk, rsW, sw);
          else dmai<(v & 3) * SLOT + 24576 + (i - 6) * 1024, (i - 6) * (8 * 3072) + 2048>(lw, vwv, rsW, sw);
        });
      } else {
        sfor<decltype(I0)::value, decltype(I1)::value>([&](auto I) {
          constexpr int i = decltype(I)::value;
          const unsigned c = (unsigned)((4 * (v - 8) + (i >> 1) + rot) & 7);
          dmai<(v - 8) * SLOT + (i >> 1) * 8192 + (i & 1) * 1024, (i & 1) * (8 * 3072)>(lw, vwk, rsW, c * (64 * 3072) + u * (16 * 3072));
        });
      }
    };
    asm volatile("s_nop 4" ::: "memory");
    STAMP(50);
    issue(IC(0), IC(0), IC(8)); issue(IC(1), IC(0), IC(8));
    MAKE_WO();
    u64 tag;
    const unsigned l0 = lane == 0 ? 0u : 0x80000000u;
    bf16x8 xf[32];
    unsigned axk[2][4];
#pragma unroll
    for (int ks = 0; ks < 4; ++ks) { axk[0][ks] = L + (32 * u + r32) * 128 + ((fx ^ (2 * ks)) << 4); axk[1][ks] = axk[0][ks] + 2 * SLOT; asm volatile("" : "+v"(axk[1][ks])); }
    sfor<0, 8>([&](auto S) {
      constexpr int s = decltype(S)::value;
      if (s == 6 && u == 0) flag_store(tag, l0, rsS, WS_FLAG + lid * 128);
      issue(IC(s + 2), IC(0), IC(3));
      WAITV(11);
      BAR();
      if (s == 0) STAMP(2); if (s == 7) STAMP(9);
      issue(IC(s + 2), IC(3), IC(4));
      if (s == 1) tag = *(const LAS u64*)(L + L_TAG);
#pragma unroll
      for (int ks = 0; ks < 4; ++ks) xf[4 * s + ks] = lds_b128(axk[(s >> 1) & 1][ks] + (s & 1) * SLOT);
      SB();
      issue(IC(s + 2), IC(4), IC(8));
    });
    cnt_arrive<0>(cb, one);
    issue_wo(IC(0));
    STAMP(10);
    bf16x8 ap[4];
#pragma unroll
    for (int a = 0; a < 4; ++a) {
      const unsigned base = L + L_PHIW + a * 512 + (8 * h + q4) * 32 + (2 * blk + (p4 >> 1)) * 8;
      const bool on = (p4 & 1) == (a & 1);
      ap[a] = lds_tr(on ? base : L + L_ZERO, on ? base + 128 : L + L_ZERO);
    }
    const unsigned of_own = WS_OF + (unsigned)(((br * 64 + 4 * rt + u) * 32 + 4 * hg) * 1024);
    cnt_wait<0>(cb, 8);
    issue_wo(IC(1));
    if (u == 0) {
      u64 fl;
      const unsigned flo = WS_FLAG + (lane < 16 ? (unsigned)((br * 16 + lane) * 8 + hg) : (unsigned)((br * 16 + rt) * 8 + (lane & 7))) * 128;
      asm volatile("buffer_load_dwordx2 %0, %1, %2, 0 offen sc1\n\ts_waitcnt vmcnt(0)" : "=&v"(fl) : "v"(flo), "s"(rsS) : "memory");
      flag_check(fl, tag, flo, rsS);
    }
    WAITV(0);
    cnt_arrive<1>(cb, one);
    STAMP(11);
    const unsigned aw0 = L + (8 * h + q4) * 128 + (((2 * blk + (p4 & 1)) ^ ((q4 >> 1) << 2)) << 4) + 8 * (p4 >> 1);
    unsigned aw1 = aw0 ^ 64u; asm volatile("" : "+v"(aw1));
    cnt_wait<1>(cb, 4);
    bf16x8 wq[3][2];
#pragma unroll
    for (int s = 0; s < 2; ++s) { wq[s][0] = lds_tr(aw0 + s * 2048, aw0 + s * 2048 + 512); wq[s][1] = lds_tr(aw1 + s * 2048, aw1 + s * 2048 + 512); }
    cnt_wait<2>(cb, 4);
    STAMP(12);
    f32x16 accQ[2] = {zero16(), zero16()};
    SB();
#pragma unroll
    for (int s = 0; s < 32; ++s) {
      if (s + 2 < 32) wq[(s + 2) % 3][0] = lds_tr(aw0 + (s + 2) * 2048, aw0 + (s + 2) * 2048 + 512);
      accQ[0] = mfma32(wq[s % 3][0], xf[s], accQ[0]);
      SB();
      if (s + 2 < 32) wq[(s + 2) % 3][1] = lds_tr(aw1 + (s + 2) * 2048, aw1 + (s + 2) * 2048 + 512);
      accQ[1] = mfma32(wq[s % 3][1], xf[s], accQ[1]);
      SB();
    }
    STAMP(13);
    bf16x8 fq[4][2];
    {
      f32x16 pq[2];
#pragma unroll
      for (int hp = 0; hp < 2; ++hp) {
        pq[hp] = mfma32(ap[2 * hp], cvt8<0>(accQ[hp]), zero16());
        pq[hp] = mfma32(ap[2 * hp + 1], cvt8<8>(accQ[hp]), pq[hp]);
      }
#pragma unroll
      for (int hp = 0; hp < 2; ++hp) {
        bf16x8 f[4];
        dual_softmax(pq[hp], f);
#pragma unroll
        for (int sg = 0; sg < 2; ++sg) {
          u32x4 a = __builtin_bit_cast(u32x4, f[2 * sg]), c = __builtin_bit_cast(u32x4, f[2 * sg + 1]);
#pragma unroll
          for (int i = 0; i < 4; ++i) { auto r = __builtin_amdgcn_permlane32_swap(a[i], c[i], false, false); a[i] = r[0]; c[i] = r[1]; }
          fq[2 * hp][sg] = __builtin_bit_cast(bf16x8, a);
          fq[2 * hp + 1][sg] = __builtin_bit_cast(bf16x8, c);
        }
      }
    }
#pragma unroll
    for (int a = 0; a < 4; ++a) asm volatile("" : "+v"(fq[a][0]), "+v"(fq[a][1]));
    unsigned kb = L + L_SCR + h * 320 + (r32 < 16 ? r32 : 16 + (r32 & 3)) * 16; asm volatile("" : "+v"(kb));
    unsigned aown = L + L_SCR + 2048 + (u & 1) * 1024 + lane * 16; asm volatile("" : "+v"(aown));
    unsigned aow = L + u * 2048 + lane * 16; asm volatile("" : "+v"(aow));
    STAMP(14);
    BAR();
    STAMP(26);
    {
      bf16x8 ka[4][2];
#pragma unroll
      for (int a = 0; a < 4; ++a) { ka[a][0] = lds_b128(kb + a * SCR); ka[a][1] = lds_b128(kb + a * SCR + 640); }
      f32x16 n[4];
#pragma unroll
      for (int a = 0; a < 4; ++a) { n[a] = mfma32(ka[a][0], fq[a][0], zero16()); n[a] = mfma32(ka[a][1], fq[a][1], n[a]); }
      u32x4 ou[4];
#pragma unroll
      for (int a = 0; a < 4; ++a) {
        const float rd = __builtin_amdgcn_rcpf(((n[a][8] + n[a][9]) + (n[a][10] + n[a][11])) + 1e-6f);
        const f32x2 r2 = {rd, rd};
        bf16x8 o;
#pragma unroll
        for (int i = 0; i < 4; ++i) { f32x2 v = f32x2{n[a][2 * i], n[a][2 * i + 1]} * r2; o[2 * i] = (__bf16)v.x; o[2 * i + 1] = (__bf16)v.y; }
        ou[a] = nopoison(o);
        st16_sc1(ou[a], (unsigned)(lane * 16), rsS, of_own + a * 1024);
      }
      if ((u >> 1) == (hg >> 2)) {
#pragma unroll
        for (int a = 0; a < 4; ++a) *(LAS u32x4*)(aown + a * SCR) = ou[a];
      }
    }
    STAMP(27);
    BAR();
    WAITV(0);
    STAMP(29);
    const unsigned of_src = WS_OF + (unsigned)(((br * 64 + 4 * rt + 2 * (hg >> 2)) * 32 + u) * 1024);
    u32x4 g[8][2];
    unsigned spins2 = 0;
    auto gload = [&](int st) {
      const unsigned so = of_src + (unsigned)(((hg + st) & 7) * 4096);
      ld16_sc1(g[st][0], (unsigned)(lane * 16), rsS, so);
      ld16_sc1(g[st][1], (unsigned)(lane * 16), rsS, so + 32768);
    };
    auto gput = [&](int st) {
      for (;;) {
        asm volatile("" : "+v"(g[st][0]), "+v"(g[st][1]));
        if (!has_poison(max(umax4(g[st][0]), umax4(g[st][1]))) || ++spins2 >= SPIN_CAP) break;
        gload(st);
        WAITV(0);
      }
      *(LAS u32x4*)(aow + (st - 1) * 8192) = g[st][0];
      *(LAS u32x4*)(aow + (st - 1) * 8192 + 1024) = g[st][1];
    };
    gload(1); gload(2); gload(3); gload(4); gload(5);
    STAMP(30);
    WAITV(8);
    STAMP(31);
    gput(1);
    BAR();
    STAMP(32);
    gload(6); gload(7);
    WAITV(10); gput(2); WAITV(8); gput(3);
    BAR();
    WAITV(6); gput(4); WAITV(4); gput(5);
    BAR();
    WAITV(2); gput(6); WAITV(0); gput(7);
    BAR();
    STAMP(38);
    STAMPV(63, spins2);
    if (rt == 0 && hg == 0 && u == 0) flag_store(tag, l0, rsS, WS_EPOCH + br * 128);
  } else {
    {
      const rsrc_t rsP = __builtin_amdgcn_make_buffer_rsrc((void*)(br ? pB : pA), 0, 32 * 512, 0x00020000);
      asm volatile("s_nop 4" ::: "memory");
      if (u < 2) dma16(L + L_PHIW + u * 1024, (unsigned)(lane * 16), rsP, (unsigned)(hg * 2048 + u * 1024));
      if (u == 3) {
        u64 e;
        asm volatile("s_load_dwordx2 %0, %1, 0x0\n\ts_waitcnt lgkmcnt(0)" : "=&s"(e) : "s"(ws + WS_EPOCH + br * 128) : "memory");
        if (lane < 3) *(LAS u32x4*)(L + L_ZERO + lane * 16) = u32x4{0, 0, 0, 0};
        if (lane == 3) *(LAS u64*)(L + L_TAG) = e + 1;
      }
    }
    MAKE_WO();
    f32x16 acc[4] = {zero16(), zero16(), zero16(), zero16()};
    unsigned axk[2][4];
#pragma unroll
    for (int ks = 0; ks < 4; ++ks) { axk[0][ks] = L + r32 * 128 + ((fx ^ (2 * ks)) << 4); axk[1][ks] = axk[0][ks] + 2 * SLOT; asm volatile("" : "+v"(axk[0][ks]), "+v"(axk[1][ks])); }
    unsigned awb[2];
    awb[0] = L + 16384 + blk * 8192 + (8 * h + q4) * 128 + (((2 * u + (p4 & 1)) ^ ((q4 >> 1) << 2) ^ (blk << 1)) << 4) + 8 * (p4 >> 1);
    awb[1] = awb[0] + 2 * SLOT; asm volatile("" : "+v"(awb[0]), "+v"(awb[1]));
    const unsigned scr = L + L_SCR + u * SCR;
    unsigned so0, so1, apa[2][2];
    {
      const unsigned dcol = 8 * ((lane >> 2) & 1) + (lane & 3) + 4 * ((lane >> 3) & 1);
      so0 = blk == 0 ? (unsigned)((u * 4 + h) * 256 + dcol * 16) : ((lane & 15) == 0 ? (unsigned)(4096 + (u * 4 + h) * 16) : 0x80000000u);
      so1 = so0 + (blk == 0 ? 512u : 32u);
      asm volatile("" : "+v"(so0), "+v"(so1));
#pragma unroll
      for (int s = 0; s < 2; ++s) {
        const unsigned base = L + L_PHIW + u * 512 + (8 * h + q4) * 32 + (2 * blk + (p4 >> 1)) * 8;
        const bool on = (p4 & 1) == s;
        apa[s][0] = on ? base : L + L_ZERO; apa[s][1] = on ? base + 128 : L + L_ZERO;
        asm volatile("" : "+v"(apa[s][0]), "+v"(apa[s][1]));
      }
    }
    bf16x8 tsel[2];
#pragma unroll
    for (int sg = 0; sg < 2; ++sg)
#pragma unroll
      for (int j = 0; j < 8; ++j) tsel[sg][j] = (r32 == 16 * sg + (j & 3) + 8 * (j >> 2) + 4 * h) ? (__bf16)1.0f : (__bf16)0.0f;
    f32x16 cone;
#pragma unroll
    for (int i = 0; i < 16; ++i) cone[i] = blk ? 1.f : 0.f;
    asm volatile("" : "+v"(tsel[0]), "+v"(tsel[1]), "+v"(cone));
    unsigned awo = L + 2 * SLOT + (4 * h + q4) * 256 + (((4 * u + 2 * (p4 & 1) + blk) ^ (q4 << 2)) << 4) + 8 * (p4 >> 1);
    unsigned gv = (unsigned)((lane >> 4) * KVP_STR + (lane & 15) * 16), gzv = (unsigned)((lane >> 2) * KVP_STR + (lane & 3) * 16);
    unsigned kw = scr + (lane >> 5) * 320 + (lane & 15) * 16 + ((lane >> 4) & 1) * 8;
    unsigned zw = lane < 32 ? scr + (lane & 3) * 320 + 256 + ((lane >> 2) & 3) * 16 + ((lane >> 4) & 1) * 8 : scr + 1280 + lane * 8;
    u16* op = out + (size_t)(rt * 128 + (hg >> 2) * 64 + (lane >> 2)) * 1024 + br * 512 + (hg & 3) * 128 + u * 32 + (lane & 3) * 8;
    asm volatile("" : "+v"(awo), "+v"(gv), "+v"(gzv), "+v"(kw), "+v"(zw), "+v"(op));
    bf16x8 sel[2];
#pragma unroll
    for (int v = 0; v < 2; ++v)
#pragma unroll
      for (int j = 0; j < 8; ++j) sel[v][j] = ((lane & 15) == 8 * v + j) ? (__bf16)1.0f : (__bf16)0.0f;
    asm volatile("" : "+v"(sel[0]), "+v"(sel[1]));
    bf16x8 fa[3], fb[3][4], ap[2];
    {
      const u32x4 poison = {0xFFFFFFFFu, 0xFFFFFFFFu, 0xFFFFFFFFu, 0xFFFFFFFFu};
      const unsigned of_own = WS_OF + (unsigned)(((br * 64 + 4 * rt + u) * 32 + 4 * hg) * 1024);
      __builtin_amdgcn_s_sleep(PSLEEP);
      st16_sc1(poison, (unsigned)(lane * 16), rsS, kvp_own + u * 1024);
      st16_sc1(poison, lane < 4 ? (unsigned)(lane * 16) : 0x80000000u, rsS, kvp_own + 4096 + u * 64);
#pragma unroll
      for (int a = 0; a < 4; ++a) st16_sc1(poison, (unsigned)(lane * 16), rsS, of_own + a * 1024);
    }
    STAMP(1);
#pragma unroll
    for (int s = 0; s < 8; ++s) {
      if (s == 1) WAITV(6);
      if (s == 2) WAITV(0);
      BAR();
      if (s == 0) STAMP(2); if (s == 7) STAMP(9);
      const unsigned sl = (s & 1) * SLOT;
#define RD_A(ks) fa[(4 * s + (ks)) % 3] = lds_tr(awb[(s >> 1) & 1] + sl + (ks) * 2048, awb[(s >> 1) & 1] + sl + (ks) * 2048 + 512)
#define RD_B(ks, t) fb[(4 * s + (ks)) % 3][t] = lds_b128(axk[(s >> 1) & 1][ks] + sl + (t) * 4096)
#define MM(n, t) acc[t] = mfma32(fa[(n) % 3], fb[(n) % 3][t], acc[t])
      SB();
      if (s > 0) {
        MM(4 * s - 1, 0); SB(); RD_A(0); RD_B(0, 0); SB();
        MM(4 * s - 1, 1); SB(); RD_B(0, 1); RD_B(0, 2); RD_B(0, 3); SB();
        MM(4 * s - 1, 2); SB(); RD_A(1); RD_B(1, 0); SB();
        MM(4 * s - 1, 3); SB(); RD_B(1, 1); RD_B(1, 2); RD_B(1, 3); SB();
      } else {
        RD_A(0); RD_B(0, 0); RD_B(0, 1); RD_B(0, 2); RD_B(0, 3); RD_A(1); RD_B(1, 0); RD_B(1, 1); RD_B(1, 2); RD_B(1, 3); SB();
      }
      MM(4 * s, 0); SB(); RD_A(2); SB();
      MM(4 * s, 1); SB(); RD_B(2, 0); RD_B(2, 1); SB();
      MM(4 * s, 2); SB(); RD_B(2, 2); RD_B(2, 3); SB();
      MM(4 * s, 3); SB();
      MM(4 * s + 1, 0); SB(); RD_A(3); SB();
      MM(4 * s + 1, 1); SB(); RD_B(3, 0); RD_B(3, 1); SB();
      MM(4 * s + 1, 2); SB(); RD_B(3, 2); RD_B(3, 3); SB();
      MM(4 * s + 1, 3); SB();
      if (s == 7) { cnt_arrive<0>(cb, one); SB(); }
      if (s == 1) { ap[0] = lds_tr(apa[0][0], apa[0][1]); ap[1] = lds_tr(apa[1][0], apa[1][1]); SB(); }
      if (s < 7) { MM(4 * s + 2, 0); MM(4 * s + 2, 1); MM(4 * s + 2, 2); MM(4 * s + 2, 3); SB(); }
    }
    MM(30, 0); MM(30, 1); MM(31, 0); MM(31, 1); SB();
    MM(30, 2); MM(30, 3); MM(31, 2); MM(31, 3);
    STAMP(10);
    __builtin_amdgcn_s_setprio(2);
    f32x16 y = zero16();
    {
      f32x16 pk[2];
#pragma unroll
      for (int gm = 0; gm < 2; ++gm) {
        pk[gm] = mfma32(ap[0], cvt8<0>(acc[2 * gm]), zero16());
        pk[gm] = mfma32(ap[1], cvt8<0>(acc[2 * gm + 1]), pk[gm]);
      }
      bf16x8 vb[4][2];
#pragma unroll
      for (int t = 0; t < 4; ++t) {
        const f32x16 tv = mfma32(cvt8<8>(acc[t]), tsel[0], cone);
        vb[t][0] = cvt8<0>(tv); vb[t][1] = cvt8<8>(tv);
      }
      f32x16 tp[4];
#pragma unroll
      for (int gm = 0; gm < 2; ++gm) {
        bf16x8 f[4];
        dual_softmax(pk[gm], f);
        if (gm == 1) { SB(); cnt_arrive<2>(cb, one); SB(); }
        bf16x8 fs[2][2];
#pragma unroll
        for (int sg = 0; sg < 2; ++sg) {
          u32x4 a = __builtin_bit_cast(u32x4, f[2 * sg]), c = __builtin_bit_cast(u32x4, f[2 * sg + 1]);
#pragma unroll
          for (int i = 0; i < 4; ++i) { auto r = __builtin_amdgcn_permlane32_swap(a[i], c[i], false, false); a[i] = r[0]; c[i] = r[1]; }
          fs[0][sg] = __builtin_bit_cast(bf16x8, a); fs[1][sg] = __builtin_bit_cast(bf16x8, c);
        }
#pragma unroll
        for (int t = 0; t < 2; ++t) {
          tp[2 * gm + t] = mfma32(fs[t][0], tsel[0], zero16());
          tp[2 * gm + t] = mfma32(fs[t][1], tsel[1], tp[2 * gm + t]);
        }
        if (gm == 0) STAMP(15);
      }
#pragma unroll
      for (int t = 0; t < 4; ++t) {
        y = mfma32(cvt8<0>(tp[t]), vb[t][0], y);
        y = mfma32(cvt8<8>(tp[t]), vb[t][1], y);
      }
    }
    st16_sc1(nopoison(cvt8<0>(y)), so0, rsS, kvp_own);
    st16_sc1(nopoison(cvt8<8>(y)), so1, rsS, kvp_own);
    STAMP(16);
    bf16x8 wf[32];
    cnt_wait<1>(cb, 4);
#pragma unroll
    for (int hd = 0; hd < 16; ++hd) wf[hd] = lds_tr(awo + hd * 4096, awo + hd * 4096 + 2048);
    cnt_arrive<4>(cb, one);
    const unsigned kvp_grp = WS_KVP + (unsigned)((br * 8 + hg) * 16) * KVP_STR;
    cnt_wait<4>(cb, 4);
    STAMP(17);
    issue_wo(IC(2)); issue_wo(IC(3));
    WAITV(16);
    STAMP(18);
    u32x4 g[4][4], gz;
    auto gq = [&](int i) {
      const unsigned so = kvp_grp + (unsigned)(((rt >> 2) + i) & 3) * (4 * KVP_STR) + u * 1024;
#pragma unroll
      for (int j = 0; j < 4; ++j) ld16_sc1(g[i][j], gv, rsS, so + j * 256);
    };
    gq(0); gq(1); gq(2); gq(3);
    ld16_sc1(gz, gzv, rsS, kvp_grp + 4096 + u * 64);
    STAMP(19);
    {
      const f32x4 z4 = {0.f, 0.f, 0.f, 0.f};
      f32x4 d[4][2], dz;
      unsigned bad = 0, spins = 0;
      auto sumq = [&](int i) {
#pragma unroll
        for (int j = 0; j < 4; ++j) asm volatile("" : "+v"(g[i][j]));
        d[i][0] = mfma16(sel[0], __builtin_bit_cast(bf16x8, g[i][0]), z4);
        d[i][1] = mfma16(sel[0], __builtin_bit_cast(bf16x8, g[i][2]), z4);
        d[i][0] = mfma16(sel[1], __builtin_bit_cast(bf16x8, g[i][1]), d[i][0]);
        d[i][1] = mfma16(sel[1], __builtin_bit_cast(bf16x8, g[i][3]), d[i][1]);
        bad &= ~(1u << i);
        if (!__all(d[i][0][0] == d[i][0][0] && d[i][1][0] == d[i][1][0]))
          if (has_poison(max(max(umax4(g[i][0]), umax4(g[i][1])), max(umax4(g[i][2]), umax4(g[i][3]))))) { bad |= 1u << i; gq(i); }
      };
      auto sumz = [&]() {
        asm volatile("" : "+v"(gz));
        dz = mfma16(sel[0], __builtin_bit_cast(bf16x8, gz), z4);
        bad &= ~16u;
        if (has_poison(umax4(gz))) { bad |= 16u; ld16_sc1(gz, gzv, rsS, kvp_grp + 4096 + u * 64); }
      };
      WAITV(13); sumq(0);
      WAITV(9); sumq(1);
      WAITV(5); sumq(2);
      WAITV(1); sumq(3);
      WAITV(0); sumz();
      while (bad && ++spins < SPIN_CAP) {
        WAITV(0);
        const unsigned was = bad;
        if (was & 1) sumq(0);
        if (was & 2) sumq(1);
        if (was & 4) sumq(2);
        if (was & 8) sumq(3);
        if (was & 16) sumz();
      }
      WAITV(0);
      STAMPV(62, spins);
      float s0[4], s1[4];
#pragma unroll
      for (int j = 0; j < 4; ++j) { s0[j] = (d[0][0][j] + d[1][0][j]) + (d[2][0][j] + d[3][0][j]); s1[j] = (d[0][1][j] + d[1][1][j]) + (d[2][1][j] + d[3][1][j]); }
      *(LAS bf16x4*)(kw) = bf16x4{(__bf16)s0[0], (__bf16)s0[1], (__bf16)s0[2], (__bf16)s0[3]};
      *(LAS bf16x4*)(kw + 640) = bf16x4{(__bf16)s1[0], (__bf16)s1[1], (__bf16)s1[2], (__bf16)s1[3]};
      *(LAS bf16x4*)(zw) = bf16x4{(__bf16)dz[0], (__bf16)dz[1], (__bf16)dz[2], (__bf16)dz[3]};
    }
    __builtin_amdgcn_s_setprio(0);
    STAMP(25);
    BAR();
    STAMP(26);
#pragma unroll
    for (int hd = 16; hd < 32; ++hd) wf[hd] = lds_tr(awo + (hd - 16) * 4096, awo + (hd - 16) * 4096 + 2048);
    unsigned aor0 = L + L_SCR + 2048 + lane * 16; asm volatile("" : "+v"(aor0));
    unsigned aor = L + lane * 16; asm volatile("" : "+v"(aor));
    unsigned ew0 = scr + r32 * 64 + (((2 * h) ^ ((r32 >> 1) & 3)) << 4), ew1 = ew0 ^ 16u, er = scr + (lane >> 2) * 64 + (((lane & 3) ^ ((lane >> 3) & 3)) << 4);
    asm volatile("" : "+v"(ew0), "+v"(ew1), "+v"(er));
    STAMP(27);
    RAWBAR();
    f32x16 c2[2] = {zero16(), zero16()};
    bf16x8 ob[64];
#pragma unroll
    for (int i = 0; i < 8; ++i) ob[i] = lds_b128(aor0 + (i >> 1) * SCR + (i & 1) * 1024);
#pragma unroll
    for (int i = 0; i < 8; ++i) c2[i & 1] = mfma32(wf[i >> 1], ob[i], c2[i & 1]);
    SB();
    STAMP(30);
#define FN(n) ((n) < 48 ? (n) : ((n) < 56 ? 48 + 2 * ((n) - 48) : 49 + 2 * ((n) - 56)))
#define RO(n) do { ob[FN(n)] = lds_b128(aor + (FN(n) - 8) * 1024); } while (0)
#define MO(n) do { c2[FN(n) & 1] = mfma32(wf[FN(n) >> 1], ob[FN(n)], c2[FN(n) & 1]); } while (0)
    RAWBAR();
    STAMP(32);
    SB();
#pragma unroll
    for (int i = 8; i < 16; ++i) RO(i);
    SB();
#pragma unroll
    for (int i = 8; i < 12; ++i) MO(i);
    SB();
#pragma unroll
    for (int gp = 0; gp < 3; ++gp) {
      RAWBAR();
      if (gp == 2) STAMP(38);
      SB();
      const int n0 = 16 + 16 * gp;
#pragma unroll
      for (int i = 0; i < 4; ++i) { MO(n0 - 4 + i); SB(); RO(n0 + 2 * i); RO(n0 + 2 * i + 1); SB(); }
#pragma unroll
      for (int i = 0; i < 8; ++i) { MO(n0 + i); SB(); RO(n0 + 8 + i); SB(); }
      if (gp < 2) {
#pragma unroll
        for (int i = 8; i < 12; ++i) MO(n0 + i);
        SB();
      }
    }
#pragma unroll
    for (int i = 56; i < 59; ++i) MO(i);
    SB();
    auto put = [&](int t) {
      *(LAS bf16x8*)(ew0 + t * 2048) = cvt8<0>(c2[t]);
      *(LAS bf16x8*)(ew1 + t * 2048) = cvt8<8>(c2[t]);
      asm volatile("" ::: "memory");
    };
    auto sto = [&](int i, bf16x8 v) {
      asm volatile("global_store_dwordx4 %0, %1, off sc1\n\ts_nop 1" :: "v"(op + (size_t)i * 16 * 1024), "v"(v) : "memory");
    };
    put(0);
    bf16x8 e0 = lds_b128(er), e1 = lds_b128(er + 1024);
    SB();
#pragma unroll
    for (int i = 59; i < 64; ++i) MO(i);
    SB();
    sto(0, e0); sto(1, e1);
    STAMP(39);
    put(1);
    e0 = lds_b128(er + 2048); e1 = lds_b128(er + 3072);
    sto(2, e0); sto(3, e1);
  }
}

extern "C" void kernel_launch(void* const* d_in, const int* in_sizes, int n_in, void* d_out, int out_size, void* d_ws, size_t ws_size, hipStream_t stream) {
  (void)hipFuncSetAttribute((const void*)hedgehog_fused, hipFuncAttributeMaxDynamicSharedMemorySize, L_BYTES);
  hedgehog_fused<<<256, 512, L_BYTES, stream>>>((const u16*)d_in[0], (const u16*)d_in[1], (const u16*)d_in[2], (const u16*)d_in[3],
                                                (const u16*)d_in[4], (const u16*)d_in[5], (const u16*)d_in[6], (const u16*)d_in[7],
                                                (u16*)d_out, (char*)d_ws);
}
```

```cpp
#include <hip/hip_runtime.h>
#include <hip/hip_bf16.h>
#include <type_traits>
#pragma clang diagnostic ignored "-Wint-to-pointer-cast"

typedef unsigned short u16;
typedef unsigned long long u64;
typedef __bf16 bf16x8 __attribute__((ext_vector_type(8)));
typedef __bf16 bf16x4 __attribute__((ext_vector_type(4)));
typedef __bf16 bf16x2 __attribute__((ext_vector_type(2)));
typedef float f32x16 __attribute__((ext_vector_type(16)));
typedef float f32x4 __attribute__((ext_vector_type(4)));
typedef float f32x2 __attribute__((ext_vector_type(2)));
typedef unsigned u32x4 __attribute__((ext_vector_type(4)));
typedef unsigned u32x2 __attribute__((ext_vector_type(2)));
#define LAS __attribute__((address_space(3)))
#define DI __device__ __forceinline__
typedef __amdgpu_buffer_rsrc_t rsrc_t;

#define NB 3
#define XNT 0
#define NB7 7
#define C3AT 6
#define HS2 4
#define PRETOUCH 1
#define HINT2 1
#define STAMPS 0

constexpr unsigned WS_EPOCH = 0;
constexpr unsigned WS_KVP   = 4096;
constexpr unsigned KVH      = 1408;
constexpr unsigned KVP_STR  = 4 * KVH;
constexpr unsigned WS_OCK   = 6u << 20;
constexpr unsigned OCK_STR  = 640;
constexpr unsigned WS_OF    = 2u << 20;
constexpr unsigned WS_BYTES = 7u << 20;
constexpr unsigned WS_STAMP = 8u << 20;
constexpr unsigned SLOT   = 32768;
constexpr unsigned L_SCR  = 2 * SLOT;
constexpr unsigned SCR    = 6144;
constexpr unsigned L_WO2  = 2 * SLOT + 28672;
constexpr unsigned L_PHIW = L_WO2 + 65536;
constexpr unsigned L_ZERO = L_PHIW + 2048;
constexpr unsigned L_CNT  = L_ZERO + 16;
constexpr unsigned L_TAG  = L_CNT + 32;
constexpr unsigned L_BYTES = 163840;
constexpr unsigned SPIN_CAP = 1u << 16;
constexpr unsigned OOB = 0x80000000u;
constexpr float L2E = 1.4426950408889634f;

#define WAITV(n) asm volatile("s_waitcnt vmcnt(" #n ")" ::: "memory")
#define WAITL0() asm volatile("s_waitcnt lgkmcnt(0)" ::: "memory")
#define RAWBAR() do { asm volatile("" ::: "memory"); __builtin_amdgcn_s_barrier(); asm volatile("" ::: "memory"); } while (0)
#define BAR() do { WAITL0(); __builtin_amdgcn_s_barrier(); asm volatile("" ::: "memory"); } while (0)
#define SB() __builtin_amdgcn_sched_barrier(0)
#define PIN(...) asm volatile("" : __VA_ARGS__)
#define UNLIKELY(x) __builtin_expect(!!(x), 0)
#define IC(n) std::integral_constant<int, n>{}
template <int I, int N, class F> DI void sfor(F&& f) { if constexpr (I < N) { f(std::integral_constant<int, I>{}); sfor<I + 1, N>(f); } }

template <int LI, int SI, bool NT = false> DI void dmai(unsigned lds, unsigned voff, rsrc_t rs, unsigned soff) {
  unsigned t;
  if constexpr (NT)
    asm volatile("s_add_u32 m0, %1, %5\n\ts_add_u32 %0, %4, %6\n\tbuffer_load_dwordx4 %2, %3, %0 offen nt lds"
                 : "=&s"(t) : "s"(lds), "v"(voff), "s"(rs), "s"(soff), "i"(LI), "i"(SI) : "memory", "scc");
  else
    asm volatile("s_add_u32 m0, %1, %5\n\ts_add_u32 %0, %4, %6\n\tbuffer_load_dwordx4 %2, %3, %0 offen lds"
                 : "=&s"(t) : "s"(lds), "v"(voff), "s"(rs), "s"(soff), "i"(LI), "i"(SI) : "memory", "scc");
}
template <int LI> DI void unit_load(u64& ck, unsigned lds, unsigned l16, unsigned lck, rsrc_t rs, unsigned so, unsigned sc) {
  asm volatile("s_add_u32 m0, %1, %7\n\ts_nop 0\n\t"
               "buffer_load_dwordx4 %2, %4, %5 offen sc1 lds\n\tbuffer_load_dwordx4 %2, %4, %5 offen offset:1024 sc1 lds\n\t"
               "buffer_load_dwordx4 %2, %4, %5 offen offset:2048 sc1 lds\n\tbuffer_load_dwordx4 %2, %4, %5 offen offset:3072 sc1 lds\n\t"
               "buffer_load_dwordx2 %0, %3, %4, %6 offen sc1"
               : "+v"(ck) : "s"(lds), "v"(l16), "v"(lck), "s"(rs), "s"(so), "s"(sc), "i"(LI) : "memory", "scc");
}
DI void quad_load(u32x4 (&g)[5], unsigned gv, rsrc_t rs, unsigned so) {
  asm volatile("buffer_load_dwordx4 %0, %5, %6, %7 offen sc1\n\tbuffer_load_dwordx4 %2, %5, %6, %7 offen offset:512 sc1\n\t"
               "buffer_load_dwordx4 %1, %5, %6, %7 offen offset:256 sc1\n\tbuffer_load_dwordx4 %3, %5, %6, %7 offen offset:768 sc1\n\t"
               "buffer_load_dwordx4 %4, %5, %6, %7 offen offset:1024 sc1"
               : "+v"(g[0]), "+v"(g[1]), "+v"(g[2]), "+v"(g[3]), "+v"(g[4]) : "v"(gv), "s"(rs), "s"(so) : "memory");
}
DI void z_load(u32x4& z, u64& zc, unsigned zv, unsigned zcv, rsrc_t rs, unsigned so) {
  asm volatile("buffer_load_dwordx4 %0, %2, %4, %5 offen offset:1280 sc1\n\tbuffer_load_dwordx2 %1, %3, %4, %5 offen offset:1312 sc1"
               : "+v"(z), "+v"(zc) : "v"(zv), "v"(zcv), "s"(rs), "s"(so) : "memory");
}
DI void hint_wait(u64 tag, unsigned hv, rsrc_t rs, unsigned& spins) {
  u64 f;
  asm volatile("buffer_load_dwordx2 %0, %1, %2, 0 offen sc1\n\ts_waitcnt vmcnt(0)" : "=&v"(f) : "v"(hv), "s"(rs) : "memory");
  if (__builtin_expect(__builtin_amdgcn_ballot_w64(f != tag) != 0, 0)) {
#pragma nounroll
    do { asm volatile("buffer_load_dwordx2 %0, %1, %2, 0 offen sc1\n\ts_waitcnt vmcnt(0)" : "=&v"(f) : "v"(hv), "s"(rs) : "memory"); }
    while (++spins < (1u << 16) && __builtin_amdgcn_ballot_w64(f != tag) != 0);
  }
}
template <int N> DI void waitv() { asm volatile("s_waitcnt vmcnt(%0)" :: "i"(N) : "memory"); }
template <int OFF> DI void st16o(u32x4 v, unsigned voff, rsrc_t rs, unsigned soff) {
  asm volatile("buffer_store_dwordx4 %0, %1, %2, %3 offen offset:%4 sc1\n\ts_nop 1" :: "v"(v), "v"(voff), "s"(rs), "s"(soff), "i"(OFF) : "memory");
}
static constexpr unsigned HC[16] = {0x5E2D58Du, 0x6B8B457u, 0x7AB3C19u, 0x4C6F2A3u, 0x71D8E95u, 0x59A40CFu, 0x63F7B61u, 0x4E1936Bu,
                                    0x7F4A7C1u, 0x54D2E87u, 0x6A09E67u, 0x43C6EF3u, 0x7254FF5u, 0x510E527u, 0x69B0569u, 0x5BE0CD1u};
DI u64 hash4(u32x4 v, u64 s, const unsigned* c) {
  u64 r;
  asm("v_mad_u64_u32 %0, vcc, %1, %6, %5\n\tv_mad_u64_u32 %0, vcc, %2, %7, %0\n\tv_mad_u64_u32 %0, vcc, %3, %8, %0\n\tv_mad_u64_u32 %0, vcc, %4, %9, %0"
      : "=&v"(r) : "v"(v[0]), "v"(v[1]), "v"(v[2]), "v"(v[3]), "v"(s), "s"(c[0]), "s"(c[1]), "s"(c[2]), "s"(c[3]) : "vcc");
  return r;
}
DI u64 hash4v(u32x4 v, u64 s, u32x4 c) {
  u64 r;
  asm("v_mad_u64_u32 %0, vcc, %1, %6, %5\n\tv_mad_u64_u32 %0, vcc, %2, %7, %0\n\tv_mad_u64_u32 %0, vcc, %3, %8, %0\n\tv_mad_u64_u32 %0, vcc, %4, %9, %0"
      : "=&v"(r) : "v"(v[0]), "v"(v[1]), "v"(v[2]), "v"(v[3]), "v"(s), "v"(c[0]), "v"(c[1]), "v"(c[2]), "v"(c[3]) : "vcc");
  return r;
}
DI u32x4 pair_halves(u64 c) {
  auto lo = __builtin_amdgcn_permlane32_swap((unsigned)c, (unsigned)c, false, false);
  auto hi = __builtin_amdgcn_permlane32_swap((unsigned)(c >> 32), (unsigned)(c >> 32), false, false);
  return u32x4{lo[0], hi[0], lo[1], hi[1]};
}
DI u64 mk64(unsigned lo, unsigned hi) { return ((u64)hi << 32) | lo; }
DI bool anyl(bool p) { return __builtin_amdgcn_ballot_w64(p) != 0; }

DI bf16x8 lds_b128(unsigned a) { return *(const LAS bf16x8*)a; }
DI bf16x8 lds_tr(unsigned a0, unsigned a1) {
  bf16x4 lo = __builtin_amdgcn_ds_read_tr16_b64_v4bf16((LAS bf16x4*)a0);
  bf16x4 hi = __builtin_amdgcn_ds_read_tr16_b64_v4bf16((LAS bf16x4*)a1);
  return __builtin_shufflevector(lo, hi, 0, 1, 2, 3, 4, 5, 6, 7);
}
DI f32x16 mfma32(bf16x8 a, bf16x8 b, f32x16 c) { return __builtin_amdgcn_mfma_f32_32x32x16_bf16(a, b, c, 0, 0, 0); }
DI f32x4 mfma16(bf16x8 a, bf16x8 b, f32x4 c) { return __builtin_amdgcn_mfma_f32_16x16x32_bf16(a, b, c, 0, 0, 0); }
DI bf16x8 asbf(u32x4 v) { return __builtin_bit_cast(bf16x8, v); }
DI u32x4 asu(bf16x8 v) { return __builtin_bit_cast(u32x4, v); }
template <int O> DI bf16x8 cvt8(const f32x16& c) {
  bf16x8 r;
#pragma unroll
  for (int i = 0; i < 8; ++i) r[i] = (__bf16)c[O + i];
  return r;
}
DI unsigned cvt2(float a, float b) { bf16x2 v = {(__bf16)a, (__bf16)b}; return __builtin_bit_cast(unsigned, v); }
DI f32x16 zero16() { f32x16 z;
#pragma unroll
  for (int i = 0; i < 16; ++i) z[i] = 0.f;
  return z; }
DI bf16x8 onehot(bool on, int j) {
  const unsigned v = on ? (0x3F80u << (16 * (j & 1))) : 0u;
  return asbf(u32x4{(j >> 1) == 0 ? v : 0u, (j >> 1) == 1 ? v : 0u, (j >> 1) == 2 ? v : 0u, (j >> 1) == 3 ? v : 0u});
}
DI void swap4(u32x4& a, u32x4& c) {
#pragma unroll
  for (int i = 0; i < 4; ++i) { auto r = __builtin_amdgcn_permlane32_swap(a[i], c[i], false, false); a[i] = r[0]; c[i] = r[1]; }
}

DI float max16(const f32x16& p) {
  float m = __builtin_elementwise_maximum(p[0], p[1]);
#pragma unroll
  for (int i = 2; i < 16; i += 2) m = __builtin_elementwise_maximum(__builtin_elementwise_maximum(m, p[i]), p[i + 1]);
  return m;
}
DI float minus_exact(const f32x16& p, float (&g)[16]) {
  float mn = p[0];
#pragma unroll
  for (int i = 1; i < 16; ++i) mn = __builtin_fminf(mn, p[i]);
  float s = 0.f;
#pragma unroll
  for (int i = 0; i < 16; ++i) { g[i] = __builtin_amdgcn_exp2f((mn - p[i]) * L2E); s += g[i]; }
  return s;
}
constexpr float BIG = 0x1p120f;
DI float sum16pk(const float (&e)[16]) {
  f32x2 t[8];
#pragma unroll
  for (int i = 0; i < 8; ++i) t[i] = f32x2{e[2 * i], e[2 * i + 1]};
  const f32x2 s = ((t[0] + t[1]) + (t[2] + t[3])) + ((t[4] + t[5]) + (t[6] + t[7]));
  return s.x + s.y;
}
DI void args16pk(const f32x16& p, float mx, float (&x)[16]) {
  const float a = -mx * L2E;
#pragma unroll
  for (int i = 0; i < 8; ++i) { const f32x2 v = f32x2{p[2 * i], p[2 * i + 1]} * f32x2{L2E, L2E} + f32x2{a, a}; x[2 * i] = v.x; x[2 * i + 1] = v.y; }
}
DI void scale16pk(float (&e)[16], float r) {
#pragma unroll
  for (int i = 0; i < 8; ++i) { const f32x2 v = f32x2{e[2 * i], e[2 * i + 1]} * f32x2{r, r}; e[2 * i] = v.x; e[2 * i + 1] = v.y; }
}
DI void pack16(const float (&e)[16], u32x4& a, u32x4& c) {
#pragma unroll
  for (int i = 0; i < 4; ++i) { a[i] = cvt2(e[2 * i], e[2 * i + 1]); c[i] = cvt2(e[8 + 2 * i], e[9 + 2 * i]); }
}
template <int I0, int I1> DI void exp_r(const float (&x)[16], float (&e)[16]) {
#pragma unroll
  for (int i = I0; i < I1; ++i) e[i] = __builtin_amdgcn_exp2f(x[i]);
}
template <int I0, int I1> DI void rcp_r(const float (&e)[16], float (&g)[16]) {
#pragma unroll
  for (int i = I0; i < I1; ++i) g[i] = __builtin_amdgcn_rcpf(e[i]);
}
DI void fma4(float (&x)[16], int o, const f32x16& p, float c, float a) {
  asm("v_fma_f32 %0, %4, %8, %9\n\tv_fma_f32 %1, %5, %8, %9\n\tv_fma_f32 %2, %6, %8, %9\n\tv_fma_f32 %3, %7, %8, %9"
      : "=&v"(x[o]), "=&v"(x[o + 1]), "=&v"(x[o + 2]), "=&v"(x[o + 3]) : "v"(p[o]), "v"(p[o + 1]), "v"(p[o + 2]), "v"(p[o + 3]), "s"(c), "v"(a));
}
DI float add4(float s, const float (&e)[16], int o) {
  float r;
  asm("v_add_f32_e32 %0, %1, %2\n\tv_add_f32_e32 %0, %0, %3\n\tv_add_f32_e32 %0, %0, %4\n\tv_add_f32_e32 %0, %0, %5"
      : "=&v"(r) : "v"(s), "v"(e[o]), "v"(e[o + 1]), "v"(e[o + 2]), "v"(e[o + 3]));
  return r;
}
DI float add3(float a, float b, float c, float d) {
  float r;
  asm("v_add_f32_e32 %0, %1, %2\n\tv_add_f32_e32 %0, %0, %3\n\tv_add_f32_e32 %0, %0, %4" : "=&v"(r) : "v"(a), "v"(b), "v"(c), "v"(d));
  return r;
}
DI u32x2 mulcvt4(float a, float b, float c, float d, float r) {
  unsigned x, y; float t0, t1;
  asm("v_mul_f32_e32 %0, %4, %8\n\tv_mul_f32_e32 %2, %5, %8\n\tv_mul_f32_e32 %1, %6, %8\n\tv_mul_f32_e32 %3, %7, %8\n\t"
      "v_cvt_pk_bf16_f32 %0, %0, %2\n\tv_cvt_pk_bf16_f32 %1, %1, %3"
      : "=&v"(x), "=&v"(y), "=&v"(t0), "=&v"(t1) : "v"(a), "v"(b), "v"(c), "v"(d), "v"(r));
  return u32x2{x, y};
}

DI u32x4 mulcvt8(const f32x16& n, float r) {
  u32x4 o; float t0, t1, t2, t3;
  asm("v_mul_f32_e32 %0, %8, %16\n\tv_mul_f32_e32 %4, %9, %16\n\tv_mul_f32_e32 %1, %10, %16\n\tv_mul_f32_e32 %5, %11, %16\n\t"
      "v_mul_f32_e32 %2, %12, %16\n\tv_mul_f32_e32 %6, %13, %16\n\tv_mul_f32_e32 %3, %14, %16\n\tv_mul_f32_e32 %7, %15, %16\n\t"
      "v_cvt_pk_bf16_f32 %0, %0, %4\n\tv_cvt_pk_bf16_f32 %1, %1, %5\n\tv_cvt_pk_bf16_f32 %2, %2, %6\n\tv_cvt_pk_bf16_f32 %3, %3, %7"
      : "=&v"(o[0]), "=&v"(o[1]), "=&v"(o[2]), "=&v"(o[3]), "=&v"(t0), "=&v"(t1), "=&v"(t2), "=&v"(t3)
      : "v"(n[0]), "v"(n[1]), "v"(n[2]), "v"(n[3]), "v"(n[4]), "v"(n[5]), "v"(n[6]), "v"(n[7]), "v"(r));
  return o;
}

#define STAMP(i) do {} while (0)
#define STAMPR(i) do {} while (0)
#define STAMPV(i, x) do {} while (0)

template <int C> DI void cnt_arrive(unsigned base, unsigned one) {
  asm volatile("s_mov_b64 exec, 1\n\tds_add_u32 %0, %1 offset:%2\n\ts_mov_b64 exec, -1" :: "v"(base), "v"(one), "i"(4 * C) : "memory");
}
template <int C, int N> DI void cnt_wait(unsigned base) {
  unsigned v, t;
  asm volatile("s_branch 2f\n1:\n\ts_sleep 1\n2:\n\tds_read_b32 %0, %2 offset:%3\n\ts_waitcnt lgkmcnt(0)\n\tv_readfirstlane_b32 %1, %0\n\ts_nop 0\n\ts_cmp_lt_u32 %1, %4\n\ts_cbranch_scc1 1b"
               : "=&v"(v), "=&s"(t) : "v"(base), "i"(4 * C), "i"(N) : "memory", "scc");
}

__global__ __launch_bounds__(512, 2) void hedgehog_fused(
    const u16* __restrict__ xA, const u16* __restrict__ wA, const u16* __restrict__ pA, const u16* __restrict__ oA,
    const u16* __restrict__ xB, const u16* __restrict__ wB, const u16* __restrict__ pB, const u16* __restrict__ oB,
    u16* __restrict__ out, char* __restrict__ ws) {
  extern __shared__ __attribute__((aligned(16))) char smem[];
  STAMPR(60); STAMP(0);
  const unsigned L = (unsigned)(size_t)(LAS char*)smem;
  const int b = blockIdx.x, xcd = b & 7, jj = b >> 3;
  const int br = xcd >> 2, rt = (xcd & 3) * 4 + (jj >> 3), hg = jj & 7;
  const int rot = (hg + 2 * (rt & 3)) & 7;
  const int tid = threadIdx.x, lane = tid & 63;
  const int w = __builtin_amdgcn_readfirstlane(tid >> 6);
  const int u = w & 3;
  const int r32 = lane & 31, h = lane >> 5, blk = (lane >> 4) & 1, q4 = (lane & 15) >> 2, p4 = lane & 3;
  const unsigned fx = (unsigned)(h ^ ((r32 >> 1) & 7));
#define MAKE_WO() \
  rsrc_t rsO = __builtin_amdgcn_make_buffer_rsrc((void*)(br ? oB : oA), 0, 512 * 1024, 0x00020000); \
  rsrc_t rsS = __builtin_amdgcn_make_buffer_rsrc((void*)ws, 0, WS_BYTES, 0x00020000); \
  PIN("+s"(rsO), "+s"(rsS)); \
  unsigned vwo = (unsigned)((lane >> 4) * 1024 + (hg & 3) * 256 + (((lane & 15) ^ ((lane >> 4) << 2)) << 4)); \
  unsigned cb = L + L_CNT, one = 1; asm volatile("" : "+v"(cb), "+v"(one), "+v"(vwo)); \
  unsigned lwo = L + u * 1024; PIN("+s"(lwo)); \
  auto issue_wo = [&](auto F0, auto F1) {     \
    sfor<decltype(F0)::value, decltype(F1)::value>([&](auto F) { constexpr int f = decltype(F)::value; \
      dmai<WO_AT(f), (f & 3) * 16384>(lwo, vwo, rsO, (unsigned)(((hg + (f >> 2)) & 7) * 65536 + u * 4096)); }); \
  };
#define WO_AT(f) ((f) < 16 ? 2 * SLOT + (f) * 4096 : ((f) < 23 ? L_WO2 + ((f) - 7) * 4096 : L_WO2 + ((f) - 23) * 4096))

  if (__builtin_expect(w >= 4, 1)) {
    const rsrc_t rsX = __builtin_amdgcn_make_buffer_rsrc((void*)((br ? xB : xA) + (size_t)rt * 128 * 512), 0, 128 * 1024, 0x00020000);
    const rsrc_t rsW = __builtin_amdgcn_make_buffer_rsrc((void*)(br ? wB : wA), 0, 512 * 3072, 0x00020000);
    const unsigned vxe = (unsigned)((lane >> 3) * 1024 + (((lane & 7) ^ (lane >> 4)) << 4));
    const unsigned vxo = vxe ^ 64u;
    const unsigned vwk = (unsigned)((lane >> 3) * 3072 + hg * 128 + (((lane & 7) ^ (((lane >> 4) & 1) << 2)) << 4));
    const unsigned vwv = vwk ^ 32u;
    const unsigned lx = L + u * 4096, lw = L + u * 2048;
    auto issue = [&](auto V, auto I0, auto I1) {
      constexpr int v = decltype(V)::value;
      if constexpr (v < 8) {
        const unsigned c = (unsigned)((v + rot) & 7);
        const unsigned sx = c * 128 + u * 32768, sw = c * (64 * 3072) + u * (16 * 3072);
        sfor<decltype(I0)::value, decltype(I1)::value>([&](auto I) {
          constexpr int i = decltype(I)::value;
          if constexpr (i < 4) dmai<(v & 3) * SLOT + i * 1024, i * 8192, (v < XNT)>(lx, (i & 1) ? vxo : vxe, rsX, sx);
          else if constexpr (i < 6) dmai<(v & 3) * SLOT + 16384 + (i - 4) * 1024, (i - 4) * (8 * 3072) + 1024>(lw, vwk, rsW, sw);
          else dmai<(v & 3) * SLOT + 24576 + (i - 6) * 1024, (i - 6) * (8 * 3072) + 2048>(lw, vwv, rsW, sw);
        });
      } else {
        sfor<decltype(I0)::value, decltype(I1)::value>([&](auto I) {
          constexpr int i = decltype(I)::value;
          const unsigned c = (unsigned)((4 * (v - 8) + (i >> 1) + rot) & 7);
          dmai<(v - 8) * SLOT + (i >> 1) * 8192 + (i & 1) * 1024, (i & 1) * (8 * 3072)>(lw, vwk, rsW, c * (64 * 3072) + u * (16 * 3072));
        });
      }
    };
    asm volatile("s_nop 4" ::: "memory");
    STAMP(50);
    issue(IC(0), IC(0), IC(8)); issue(IC(1), IC(0), IC(8));
    MAKE_WO();
    u64 tag;
    bf16x8 xf[32], ap[4];
    unsigned axk[2][4];
#pragma unroll
    for (int ks = 0; ks < 4; ++ks) { axk[0][ks] = L + (32 * u + r32) * 128 + ((fx ^ (2 * ks)) << 4); axk[1][ks] = axk[0][ks] + 2 * SLOT; asm volatile("" : "+v"(axk[1][ks])); }
    unsigned aw0 = L + (8 * h + q4) * 128 + (((2 * blk + (p4 & 1)) ^ ((q4 >> 1) << 2)) << 4) + 8 * (p4 >> 1), aw1 = aw0 ^ 64u;
    unsigned apb = L + L_PHIW + (8 * h + q4) * 32 + (2 * blk + (p4 >> 1)) * 8;
    asm volatile("" : "+v"(aw0), "+v"(aw1), "+v"(apb));
    sfor<0, 8>([&](auto S) {
      constexpr int s = decltype(S)::value;
      if constexpr (s < 7) { issue(IC(s + 2), IC(0), IC(NB)); waitv<8 + NB>(); }
      else waitv<NB7>();
      BAR();
      if (s == 0) { STAMP(2); STAMPR(52); } if (s == 7) { STAMP(9); STAMPR(53); }
      if constexpr (s < 6) issue(IC(s + 2), IC(NB), IC(8));
      if constexpr (s == 6) issue(IC(8), IC(NB), IC(NB7));
      if constexpr (s == 7) { issue(IC(8), IC(NB7), IC(8)); issue(IC(9), IC(0), IC(5)); }
      if (s == 1) tag = *(const LAS u64*)(L + L_TAG);
      if constexpr (s >= 2 && s < 6) {
        const bool on = (p4 & 1) == (s & 1);
        ap[s - 2] = lds_tr(on ? apb + (s - 2) * 512 : L + L_ZERO, on ? apb + (s - 2) * 512 + 128 : L + L_ZERO);
      }
#pragma unroll
      for (int ks = 0; ks < 4; ++ks) xf[4 * s + ks] = lds_b128(axk[(s >> 1) & 1][ks] + (s & 1) * SLOT);
      SB();
    });
    cnt_arrive<0>(cb, one);
    issue(IC(9), IC(5), IC(8));
    issue_wo(IC(0), IC(8));
    cnt_wait<0, 8>(cb);
    issue_wo(IC(8), IC(16));
    STAMP(10);
    waitv<16>();
    cnt_arrive<1>(cb, one);
    unsigned hc[16];
#pragma unroll
    for (int i = 0; i < 16; ++i) { hc[i] = HC[i]; asm volatile("" : "+s"(hc[i])); }
    unsigned kb = L + L_SCR + h * 320 + (r32 < 16 ? r32 : 16 + (r32 & 3)) * 16;
    unsigned aown = L + L_SCR + 2048 + (u & 1) * 1024 + lane * 16;
    unsigned l16 = lane * 16, l16h = lane < 40 ? l16 : OOB, lck = r32 * 16 + h * 8, avf = lx + lane * 16;
    asm volatile("" : "+v"(kb), "+v"(aown), "+v"(l16), "+v"(l16h), "+v"(lck), "+v"(avf));
    unsigned gso[4], gsc[4], hv;
    u64 pre = 0;
    {
      const unsigned tt2 = (unsigned)(br * 64 + 4 * rt + 2 * (hg >> 2) + (u & 1));
#pragma unroll
      for (int g = 0; g < 4; ++g) {
        const unsigned pr = (unsigned)(hg + 2 * g + 1 + (u >> 1)) & 7;
        gso[g] = WS_OF + (tt2 * 32 + 4 * pr) * 1024; gsc[g] = WS_OCK + (tt2 * 8 + pr) * OCK_STR;
        asm volatile("" : "+s"(gso[g]), "+s"(gsc[g]));
      }
      hv = WS_OCK + (tt2 * 8 + ((unsigned)(hg + 2 * (u < 2 ? lane & 3 : lane % 3u) + 1 + (u >> 1)) & 7)) * OCK_STR + 512;
      PIN("+v"(hv));
    }
    unsigned of_own = WS_OF + (unsigned)(((br * 64 + 4 * rt + u) * 32 + 4 * hg) * 1024), ock = WS_OCK + (unsigned)((br * 64 + 4 * rt + u) * 8 + hg) * OCK_STR;
    asm volatile("" : "+s"(of_own), "+s"(ock));
    { u64 f; asm volatile("buffer_load_dwordx2 %0, %1, %2, 0 offen sc1" : "=v"(f) : "v"(hv), "s"(rsS) : "memory"); pre = f; }
    STAMP(11);
    cnt_wait<1, 4>(cb);
    bf16x8 wq[4];
#define RQ(n) wq[(n) & 3] = lds_tr(((n) < 32 ? aw0 : aw1) + ((n) & 31) * 2048, ((n) < 32 ? aw0 : aw1) + ((n) & 31) * 2048 + 512)
    RQ(0); RQ(1); RQ(2);
    cnt_wait<2, 4>(cb);
    __builtin_amdgcn_s_setprio(1);
    STAMP(12);
    f32x16 q0 = zero16(), q1 = zero16();
    SB();
#pragma unroll
    for (int n = 0; n < 32; ++n) {
      RQ(n + 3); q0 = mfma32(wq[n & 3], xf[n], q0); SB();
      if (n == C3AT) { waitv<PRETOUCH>(); cnt_arrive<3>(cb, one); SB(); }
    }
    u32x4 fq[4][2];
    {
      f32x16 p;
      float x[16], e[16], g[16], ca, se, sg, re, rg;
      bf16x8 c0, c1;
      sfor<32, 64>([&](auto N) {
        constexpr int n = decltype(N)::value, i = n - 32;
        if constexpr (n + 3 < 64) RQ(n + 3);
        q1 = mfma32(wq[n & 3], xf[i], q1);
        SB();
        if constexpr (i == 2) c0 = cvt8<0>(q0);
        if constexpr (i == 3) { c1 = cvt8<8>(q0); p = mfma32(ap[0], c0, zero16()); }
        if constexpr (i == 4) p = mfma32(ap[1], c1, p);
        if constexpr (i == 8) ca = -max16(p) * L2E;
        if constexpr (i == 9) { fma4(x, 0, p, L2E, ca); fma4(x, 4, p, L2E, ca); }
        if constexpr (i == 10) { exp_r<0, 3>(x, e); }
        if constexpr (i == 11) { fma4(x, 8, p, L2E, ca); exp_r<3, 5>(x, e); }
        if constexpr (i == 12) { fma4(x, 12, p, L2E, ca); exp_r<5, 7>(x, e); }
        if constexpr (i >= 13 && i < 16) exp_r<7 + 3 * (i - 13), 10 + 3 * (i - 13)>(x, e);
        if constexpr (i == 16) { rcp_r<0, 2>(e, g); se = add3(e[0], e[1], e[2], e[3]); }
        if constexpr (i >= 17 && i < 20) { rcp_r<2 * (i - 16), 2 * (i - 15)>(e, g); se = add4(se, e, 4 * (i - 16)); }
        if constexpr (i == 20) { re = __builtin_amdgcn_rcpf(se); PIN("+v"(re)); rcp_r<8, 10>(e, g); sg = add3(g[0], g[1], g[2], g[3]); }
        if constexpr (i == 21) { rcp_r<10, 12>(e, g); sg = add4(sg, g, 4); }
        if constexpr (i == 22) { rcp_r<12, 16>(e, g); }
        if constexpr (i == 23) { sg = add4(sg, g, 8); sg = add4(sg, g, 12); }
        if constexpr (i == 24) { if (UNLIKELY(anyl(!(sg < BIG)))) sg = minus_exact(p, g); rg = __builtin_amdgcn_rcpf(sg); }
        if constexpr (i >= 24 && i < 28) { constexpr int k = i - 24; const u32x2 t = mulcvt4(e[4 * k], e[4 * k + 1], e[4 * k + 2], e[4 * k + 3], re); fq[k >> 1][0][2 * (k & 1)] = t.x; fq[k >> 1][0][2 * (k & 1) + 1] = t.y; }
        if constexpr (i >= 28) { constexpr int k = i - 28; const u32x2 t = mulcvt4(g[4 * k], g[4 * k + 1], g[4 * k + 2], g[4 * k + 3], rg); fq[k >> 1][1][2 * (k & 1)] = t.x; fq[k >> 1][1][2 * (k & 1) + 1] = t.y; }
        SB();
      });
      STAMP(13);
      asm volatile("s_nop 1" : "+v"(fq[0][0]), "+v"(fq[1][0]), "+v"(fq[0][1]), "+v"(fq[1][1]));
      swap4(fq[0][0], fq[1][0]);
      SB();
      c0 = cvt8<0>(q1); c1 = cvt8<8>(q1);
      p = mfma32(ap[2], c0, zero16());
      p = mfma32(ap[3], c1, p);
      SB();
      swap4(fq[0][1], fq[1][1]);
      SB();
      args16pk(p, max16(p), x);
      exp_r<0, 16>(x, e); rcp_r<0, 16>(e, g);
      se = sum16pk(e); sg = sum16pk(g); PIN("+v"(se));
      if (UNLIKELY(anyl(!(sg < BIG)))) sg = minus_exact(p, g);
      scale16pk(e, __builtin_amdgcn_rcpf(se)); scale16pk(g, __builtin_amdgcn_rcpf(sg));
      pack16(e, fq[2][0], fq[3][0]); pack16(g, fq[2][1], fq[3][1]);
      swap4(fq[2][0], fq[3][0]); swap4(fq[2][1], fq[3][1]);
    }
#pragma unroll
    for (int a = 0; a < 4; ++a) asm volatile("" : "+v"(fq[a][0]), "+v"(fq[a][1]));
    __builtin_amdgcn_s_setprio(3);
    WAITV(0);
    asm volatile("" :: "v"(pre));
    STAMP(14);
    RAWBAR();
    STAMP(26); STAMPR(55);
    {
      bf16x8 ka[4][2];
#pragma unroll
      for (int a = 0; a < 4; ++a) { ka[a][0] = lds_b128(kb + a * SCR); ka[a][1] = lds_b128(kb + a * SCR + 640); }
      SB();
      f32x16 n[4];
      u32x4 ou[4];
      u64 hs = tag;
      auto mm = [&](int a) { n[a] = mfma32(ka[a][0], asbf(fq[a][0]), zero16()); n[a] = mfma32(ka[a][1], asbf(fq[a][1]), n[a]); SB(); };
      float rd[4];
      auto den = [&](int a) { rd[a] = __builtin_amdgcn_rcpf((((n[a][8] + n[a][9]) + n[a][10]) + n[a][11]) + 1e-6f); SB(); };
      auto fin = [&](auto A) { constexpr int a = decltype(A)::value; ou[a] = mulcvt8(n[a], rd[a]); st16o<a * 1024>(ou[a], l16, rsS, of_own); SB(); };
      mm(0); mm(1); mm(2);
      den(0);
      mm(3);
      fin(IC(0)); den(1); hs = hash4(ou[0], hs, hc); SB();
      fin(IC(1)); den(2); hs = hash4(ou[1], hs, hc + 4); SB();
      fin(IC(2)); den(3); hs = hash4(ou[2], hs, hc + 8); SB();
      fin(IC(3)); hs = hash4(ou[3], hs, hc + 12);
      asm volatile("" :: "v"(n[0]), "v"(n[1]), "v"(n[2]), "v"(n[3]));
      u32x4 cw = pair_halves(hs);
      cw[0] = h ? (unsigned)tag : cw[0]; cw[1] = h ? (unsigned)(tag >> 32) : cw[1];
      st16o<0>(cw, l16h, rsS, ock);
      if ((u >> 1) == (hg >> 2)) {
#pragma unroll
        for (int a = 0; a < 4; ++a) *(LAS u32x4*)(aown + a * SCR) = ou[a];
      }
    }
    STAMP(27);
    BAR();
    unsigned spins2 = 0;
    __builtin_amdgcn_s_sleep(HS2);
    STAMP(28);
    hint_wait(tag, hv, rsS, spins2);
    STAMP(29);
    u64 ck[4];
    asm volatile("" : "=v"(ck[0]), "=v"(ck[1]), "=v"(ck[2]), "=v"(ck[3]));
    auto verify = [&](auto G) {
      constexpr int g = decltype(G)::value;
      auto bad = [&]() {
        asm volatile("" : "+v"(ck[g]) :: "memory");
        const u32x4 v0 = *(const LAS u32x4*)(avf + g * 16384), v1 = *(const LAS u32x4*)(avf + g * 16384 + 1024);
        const u32x4 v2 = *(const LAS u32x4*)(avf + g * 16384 + 2048), v3 = *(const LAS u32x4*)(avf + g * 16384 + 3072);
        return anyl(hash4(v3, hash4(v2, hash4(v1, hash4(v0, tag, hc), hc + 4), hc + 8), hc + 12) != ck[g]);
      };
      if (UNLIKELY(bad())) {
#pragma nounroll
        do { unit_load<g * 16384>(ck[g], lx, l16, lck, rsS, gso[g], gsc[g]); WAITV(0); } while (++spins2 < SPIN_CAP && bad());
      }
    };
    unit_load<0>(ck[0], lx, l16, lck, rsS, gso[0], gsc[0]);
    unit_load<16384>(ck[1], lx, l16, lck, rsS, gso[1], gsc[1]);
    STAMP(30);
    waitv<5>();
    STAMP(31);
    unit_load<32768>(ck[2], lx, l16, lck, rsS, gso[2], gsc[2]);
    verify(IC(0));
    RAWBAR();
    STAMP(32); STAMPR(56);
    if (u < 2) unit_load<49152>(ck[3], lx, l16, lck, rsS, gso[3], gsc[3]);
    if (u < 2) waitv<10>(); else waitv<5>();
    verify(IC(1));
    STAMP(33);
    RAWBAR();
    if (u < 2) waitv<5>(); else waitv<0>();
    verify(IC(2));
    STAMP(34);
    RAWBAR();
    if (u < 2) { waitv<0>(); verify(IC(3)); }
    STAMP(35);
    RAWBAR();
    STAMP(38);
    STAMPV(63, spins2);
    if (rt == 0 && hg == 0 && u == 0)
      asm volatile("buffer_store_dwordx2 %0, %1, %2, %3 offen sc1" :: "v"(tag), "v"(lane == 0 ? 0u : OOB), "s"(rsS), "s"(WS_EPOCH + br * 128) : "memory");
  } else {
    {
      const rsrc_t rsP = __builtin_amdgcn_make_buffer_rsrc((void*)(br ? pB : pA), 0, 32 * 512, 0x00020000);
      asm volatile("s_nop 4" ::: "memory");
      if (u < 2) dmai<L_PHIW, 0>(L + u * 1024, (unsigned)(lane * 16), rsP, (unsigned)(hg * 2048 + u * 1024));
      if (u == 3) {
        u64 e;
        asm volatile("s_load_dwordx2 %0, %1, 0x0\n\ts_waitcnt lgkmcnt(0)" : "=&s"(e) : "s"(ws + WS_EPOCH + br * 128) : "memory");
        if (lane < 3) *(LAS u32x4*)(L + L_ZERO + lane * 16) = u32x4{0, 0, 0, 0};
        if (lane == 3) *(LAS u64*)(L + L_TAG) = e + 1;
      }
    }
    MAKE_WO();
    f32x16 acc[4] = {zero16(), zero16(), zero16(), zero16()};
    unsigned axk[2][4];
#pragma unroll
    for (int ks = 0; ks < 4; ++ks) { axk[0][ks] = L + r32 * 128 + ((fx ^ (2 * ks)) << 4); axk[1][ks] = axk[0][ks] + 2 * SLOT; asm volatile("" : "+v"(axk[0][ks]), "+v"(axk[1][ks])); }
    unsigned awb[2];
    awb[0] = L + 16384 + blk * 8192 + (8 * h + q4) * 128 + (((2 * u + (p4 & 1)) ^ ((q4 >> 1) << 2) ^ (blk << 1)) << 4) + 8 * (p4 >> 1);
    awb[1] = awb[0] + 2 * SLOT; asm volatile("" : "+v"(awb[0]), "+v"(awb[1]));
    const unsigned scr = L + L_SCR + u * SCR;
    const int jt = (lane & 3) + 4 * ((lane >> 3) & 1);
    unsigned so0, so2, apa[2][2];
    {
      const unsigned dcol = 8 * ((lane >> 2) & 1) + jt;
      so0 = blk == 0 ? h * 256 + dcol * 16 : OOB;
      so2 = blk == 0 ? (h ? OOB : 1024 + dcol * 16) : ((lane & 15) < 4 ? 1280u + h * 64 + p4 * 16 : OOB);
      asm volatile("" : "+v"(so0), "+v"(so2));
#pragma unroll
      for (int s = 0; s < 2; ++s) {
        const unsigned base = L + L_PHIW + u * 512 + (8 * h + q4) * 32 + (2 * blk + (p4 >> 1)) * 8;
        const bool on = (p4 & 1) == s;
        apa[s][0] = on ? base : L + L_ZERO; apa[s][1] = on ? base + 128 : L + L_ZERO;
        asm volatile("" : "+v"(apa[s][0]), "+v"(apa[s][1]));
      }
    }
    bf16x8 tsel[2] = {onehot(((lane >> 2) & 1) == h && blk == 0, jt), onehot(((lane >> 2) & 1) == h && blk == 1, jt)};
    bf16x8 sel[2] = {onehot(((lane >> 3) & 1) == 0, lane & 7), onehot(((lane >> 3) & 1) == 1, lane & 7)};
    f32x16 cone;
#pragma unroll
    for (int i = 0; i < 16; ++i) cone[i] = blk ? 1.f : 0.f;
    asm volatile("" : "+v"(tsel[0]), "+v"(tsel[1]), "+v"(sel[0]), "+v"(sel[1]), "+v"(cone));
    unsigned awo = L + 2 * SLOT + (4 * h + q4) * 256 + (((4 * u + 2 * (p4 & 1) + blk) ^ (q4 << 2)) << 4) + 8 * (p4 >> 1);
    unsigned awo2 = awo + (L_WO2 - 2 * SLOT); PIN("+v"(awo2));
    unsigned gv = (unsigned)((lane >> 4) * KVP_STR + (lane & 15) * 16);
    unsigned zv = (unsigned)((lane >> 2) * KVP_STR + ((lane >> 1) & 1) * 64 + (lane & 1) * 16), zcv = zv - (lane & 1) * 8;
    unsigned kw = scr + h * 320 + (lane & 15) * 16 + blk * 8;
    unsigned zw = h ? scr + 1280 + lane * 8 : scr + (2 * (lane & 1) + ((lane >> 1) & 1)) * 320 + 256 + ((lane >> 2) & 3) * 16 + blk * 8;
    unsigned ov = (unsigned)((lane >> 2) * 2048 + (lane & 3) * 16);
    rsrc_t rsY = __builtin_amdgcn_make_buffer_rsrc((void*)(out + (size_t)(rt * 128 + (hg >> 2) * 64) * 1024 + br * 512 + (hg & 3) * 128 + u * 32), 0, 63 * 2048 + 64, 0x00020000);
    asm volatile("" : "+v"(awo), "+v"(gv), "+v"(zv), "+v"(zcv), "+v"(kw), "+v"(zw), "+v"(ov), "+s"(rsY));
    unsigned kvp_grp = WS_KVP + (unsigned)((br * 8 + hg) * 16) * KVP_STR + u * KVH, kvp_own = kvp_grp + rt * KVP_STR;
    unsigned qso[4], hc[8];
#pragma unroll
    for (int i = 0; i < 4; ++i) { qso[i] = kvp_grp + (unsigned)(((rt >> 2) + i) & 3) * (4 * KVP_STR); asm volatile("" : "+s"(qso[i])); }
#pragma unroll
    for (int i = 0; i < 8; ++i) { hc[i] = HC[i]; asm volatile("" : "+s"(hc[i])); }
    u32x4 zhc;
#pragma unroll
    for (int i = 0; i < 4; ++i) zhc[i] = HC[i] + (unsigned)(lane & 1) * (HC[4 + i] - HC[i]);
    asm volatile("" : "+s"(kvp_grp), "+s"(kvp_own), "+v"(zhc));
    u64 tag;
    bf16x8 fa[3], fb[3][4], ap[2];
    WAITV(0);
    STAMP(1);
#pragma unroll
    for (int s = 0; s < 8; ++s) {
      BAR();
      if (s == 0) STAMP(2); if (s == 7) STAMP(9);
      const unsigned sl = (s & 1) * SLOT;
#define RD_A(ks) fa[(4 * s + (ks)) % 3] = lds_tr(awb[(s >> 1) & 1] + sl + (ks) * 2048, awb[(s >> 1) & 1] + sl + (ks) * 2048 + 512)
#define RD_B(ks, t) fb[(4 * s + (ks)) % 3][t] = lds_b128(axk[(s >> 1) & 1][ks] + sl + (t) * 4096)
#define MM(n, t) acc[t] = mfma32(fa[(n) % 3], fb[(n) % 3][t], acc[t])
      SB();
      if (s > 0) {
        MM(4 * s - 1, 0); SB(); RD_A(0); RD_B(0, 0); SB();
        MM(4 * s - 1, 1); SB(); RD_B(0, 1); RD_B(0, 2); RD_B(0, 3); SB();
        MM(4 * s - 1, 2); SB(); RD_A(1); RD_B(1, 0); SB();
        MM(4 * s - 1, 3); SB(); RD_B(1, 1); RD_B(1, 2); RD_B(1, 3); SB();
      } else {
        RD_A(0); RD_B(0, 0); RD_B(0, 1); RD_B(0, 2); RD_B(0, 3); RD_A(1); RD_B(1, 0); RD_B(1, 1); RD_B(1, 2); RD_B(1, 3); SB();
      }
      MM(4 * s, 0); SB(); RD_A(2); SB();
      MM(4 * s, 1); SB(); RD_B(2, 0); RD_B(2, 1); SB();
      MM(4 * s, 2); SB(); RD_B(2, 2); RD_B(2, 3); SB();
      MM(4 * s, 3); SB();
      MM(4 * s + 1, 0); SB(); RD_A(3); SB();
      MM(4 * s + 1, 1); SB(); RD_B(3, 0); RD_B(3, 1); SB();
      MM(4 * s + 1, 2); SB(); RD_B(3, 2); RD_B(3, 3); SB();
      MM(4 * s + 1, 3); SB();
      if (s == 7) { cnt_arrive<0>(cb, one); SB(); }
      if (s == 1) { ap[0] = lds_tr(apa[0][0], apa[0][1]); ap[1] = lds_tr(apa[1][0], apa[1][1]); tag = *(const LAS u64*)(L + L_TAG); SB(); }
      if (s < 7) { MM(4 * s + 2, 0); MM(4 * s + 2, 1); MM(4 * s + 2, 2); MM(4 * s + 2, 3); SB(); }
    }
    __builtin_amdgcn_s_setprio(2);
    f32x16 y;
    {
      bf16x8 kc[4], vc[4], vb[4][2];
      f32x16 p0, p1, tv[4], tp[2];
      float x[16], e[16], g[16], x1[16], e1[16], g1[16];
      u32x4 fp[2], fm[2];
      MM(30, 0); MM(31, 0); MM(30, 1); MM(31, 1); MM(30, 2); SB();
      kc[0] = cvt8<0>(acc[0]); SB(); MM(31, 2); SB();
      vc[0] = cvt8<8>(acc[0]); SB(); MM(30, 3); SB();
      kc[1] = cvt8<0>(acc[1]); SB(); MM(31, 3); SB();
      vc[1] = cvt8<8>(acc[1]); SB();
      p0 = mfma32(ap[0], kc[0], zero16()); SB(); kc[2] = cvt8<0>(acc[2]); SB();
      p0 = mfma32(ap[1], kc[1], p0); SB(); vc[2] = cvt8<8>(acc[2]); SB();
      tv[0] = mfma32(vc[0], tsel[0], cone); SB(); kc[3] = cvt8<0>(acc[3]); SB();
      tv[1] = mfma32(vc[1], tsel[0], cone); SB(); vc[3] = cvt8<8>(acc[3]); SB();
      p1 = mfma32(ap[0], kc[2], zero16()); SB();
      const float m0 = max16(p0); SB();
      p1 = mfma32(ap[1], kc[3], p1); SB(); vb[0][0] = cvt8<0>(tv[0]); vb[0][1] = cvt8<8>(tv[0]); PIN("+v"(vb[0][0]), "+v"(vb[0][1])); SB();
      tv[2] = mfma32(vc[2], tsel[0], cone); SB(); vb[1][0] = cvt8<0>(tv[1]); vb[1][1] = cvt8<8>(tv[1]); PIN("+v"(vb[1][0]), "+v"(vb[1][1])); SB();
      tv[3] = mfma32(vc[3], tsel[0], cone); SB();
      float m1 = max16(p1); PIN("+v"(m1)); SB();
      vb[2][0] = cvt8<0>(tv[2]); vb[2][1] = cvt8<8>(tv[2]); PIN("+v"(vb[2][0]), "+v"(vb[2][1])); SB();
      args16pk(p0, m0, x); args16pk(p1, m1, x1); SB();
      exp_r<0, 16>(x, e); vb[3][0] = cvt8<0>(tv[3]); vb[3][1] = cvt8<8>(tv[3]); PIN("+v"(vb[3][0]), "+v"(vb[3][1])); rcp_r<0, 16>(e, g); SB();
      float se = sum16pk(e), sg = sum16pk(g); PIN("+v"(se));
      if (UNLIKELY(anyl(!(sg < BIG)))) sg = minus_exact(p0, g);
      scale16pk(e, __builtin_amdgcn_rcpf(se)); scale16pk(g, __builtin_amdgcn_rcpf(sg)); SB();
      STAMP(15);
      pack16(e, fp[0], fp[1]); swap4(fp[0], fp[1]); SB();
      tp[0] = mfma32(asbf(fp[0]), tsel[0], zero16()); SB(); pack16(g, fm[0], fm[1]); SB();
      tp[1] = mfma32(asbf(fp[1]), tsel[0], zero16()); SB(); swap4(fm[0], fm[1]); SB();
      tp[0] = mfma32(asbf(fm[0]), tsel[1], tp[0]); SB(); exp_r<0, 4>(x1, e1); SB();
      tp[1] = mfma32(asbf(fm[1]), tsel[1], tp[1]); SB(); exp_r<4, 16>(x1, e1); SB();
      kc[0] = cvt8<0>(tp[0]); kc[1] = cvt8<8>(tp[0]); SB();
      y = mfma32(kc[0], vb[0][0], zero16()); SB(); kc[2] = cvt8<0>(tp[1]); kc[3] = cvt8<8>(tp[1]); SB();
      y = mfma32(kc[1], vb[0][1], y); SB(); rcp_r<0, 4>(e1, g1); SB();
      y = mfma32(kc[2], vb[1][0], y); SB(); rcp_r<4, 8>(e1, g1); SB();
      y = mfma32(kc[3], vb[1][1], y); SB(); rcp_r<8, 16>(e1, g1); SB();
      se = sum16pk(e1); sg = sum16pk(g1); PIN("+v"(se));
      if (UNLIKELY(anyl(!(sg < BIG)))) sg = minus_exact(p1, g1);
      scale16pk(e1, __builtin_amdgcn_rcpf(se)); scale16pk(g1, __builtin_amdgcn_rcpf(sg)); SB();
      cnt_arrive<2>(cb, one); SB();
      pack16(e1, fp[0], fp[1]); swap4(fp[0], fp[1]); SB();
      tp[0] = mfma32(asbf(fp[0]), tsel[0], zero16()); SB(); pack16(g1, fm[0], fm[1]); SB();
      tp[1] = mfma32(asbf(fp[1]), tsel[0], zero16()); SB(); swap4(fm[0], fm[1]); SB();
      tp[0] = mfma32(asbf(fm[0]), tsel[1], tp[0]);
      tp[1] = mfma32(asbf(fm[1]), tsel[1], tp[1]); SB();
      kc[0] = cvt8<0>(tp[0]); SB();
      y = mfma32(kc[0], vb[2][0], y); SB(); kc[1] = cvt8<8>(tp[0]); SB();
      y = mfma32(kc[1], vb[2][1], y); SB(); kc[2] = cvt8<0>(tp[1]); SB();
      y = mfma32(kc[2], vb[3][0], y); SB(); kc[3] = cvt8<8>(tp[1]); SB();
      y = mfma32(kc[3], vb[3][1], y); SB();
    }
    {
      const u32x4 yp = asu(cvt8<0>(y)), ym = asu(cvt8<8>(y));
      st16o<0>(yp, so0, rsS, kvp_own);
      st16o<512>(ym, so0, rsS, kvp_own);
      const u64 hp = hash4(yp, tag, hc), hm = hash4(ym, tag, hc + 4);
      const u32x4 pr = pair_halves(hp + hm), hz = {(unsigned)hp, (unsigned)(hp >> 32), (unsigned)hm, (unsigned)(hm >> 32)};
      u32x4 cw;
#pragma unroll
      for (int i = 0; i < 4; ++i) { const unsigned zz = (lane & 1) ? ym[i] : yp[i], cc = blk ? hz[i] : pr[i]; cw[i] = (lane & 18) == 16 ? zz : cc; }
      st16o<0>(cw, so2, rsS, kvp_own);
    }
    STAMP(16); STAMPR(54);
    issue_wo(IC(16), IC(23));
    bf16x8 wf[32];
#define RW(f) wf[f] = lds_tr(((f) < 16 ? awo : awo2) + (WO_AT(f) - ((f) < 16 ? 2 * SLOT : L_WO2)), ((f) < 16 ? awo : awo2) + (WO_AT(f) - ((f) < 16 ? 2 * SLOT : L_WO2)) + 2048)
    cnt_wait<3, 4>(cb);
#pragma unroll
    for (int hd = 0; hd < 16; ++hd) RW(hd);
    cnt_arrive<4>(cb, one);
    STAMP(17);
    unsigned spins = 0;
    waitv<7>();
    STAMP(18);
    {
      u32x4 g[4][5], zp;
      u64 zc;
#pragma unroll
      for (int i = 0; i < 4; ++i) asm volatile("" : "=v"(g[i][0]), "=v"(g[i][1]), "=v"(g[i][2]), "=v"(g[i][3]), "=v"(g[i][4]));
      asm volatile("" : "=v"(zp), "=v"(zc));
      quad_load(g[0], gv, rsS, qso[0]); quad_load(g[1], gv, rsS, qso[1]); quad_load(g[2], gv, rsS, qso[2]);
      z_load(zp, zc, zv, zcv, rsS, kvp_grp);
      quad_load(g[3], gv, rsS, qso[3]);
      const u64 tag2 = tag + tag;
      cnt_wait<4, 4>(cb);
      STAMP(19);
      const f32x4 z4 = {0.f, 0.f, 0.f, 0.f};
      f32x4 d[4][2], dz;
      unsigned bad = 0;
      auto sumq = [&](int i) {
        asm volatile("" : "+v"(g[i][0]), "+v"(g[i][1]), "+v"(g[i][2]), "+v"(g[i][3]), "+v"(g[i][4]));
        d[i][0] = mfma16(sel[0], asbf(g[i][0]), z4);
        d[i][1] = mfma16(sel[0], asbf(g[i][2]), z4);
        d[i][0] = mfma16(sel[1], asbf(g[i][1]), d[i][0]);
        d[i][1] = mfma16(sel[1], asbf(g[i][3]), d[i][1]);
        SB();
        const u64 h0 = hash4(g[i][2], hash4(g[i][0], tag2, hc), hc + 4), h1 = hash4(g[i][3], hash4(g[i][1], tag2, hc), hc + 4);
        if (UNLIKELY(anyl(h0 != mk64(g[i][4][0], g[i][4][1]) || h1 != mk64(g[i][4][2], g[i][4][3])))) { bad |= 1u << i; quad_load(g[i], gv, rsS, qso[i]); }
      };
      auto sumz = [&]() {
        asm volatile("" : "+v"(zp), "+v"(zc));
        dz = mfma16(sel[0], asbf(zp), z4);
        SB();
        if (UNLIKELY(anyl(hash4v(zp, tag, zhc) != zc))) { bad |= 16u; z_load(zp, zc, zv, zcv, rsS, kvp_grp); }
      };
      WAITV(17); sumq(0);
      WAITV(12); sumq(1);
      WAITV(7); sumq(2);
      WAITV(5); sumz();
      WAITV(0); sumq(3);
      if (UNLIKELY(bad)) {
#pragma nounroll
        do {
          WAITV(0);
          const unsigned was = bad; bad = 0;
          if (was & 1) sumq(0);
          if (was & 2) sumq(1);
          if (was & 4) sumq(2);
          if (was & 8) sumq(3);
          if (was & 16) sumz();
        } while (bad && ++spins < SPIN_CAP);
        WAITV(0);
      }
      STAMPV(62, spins);
      float s0[4], s1[4];
#pragma unroll
      for (int j = 0; j < 4; ++j) { s0[j] = add3(d[0][0][j], d[1][0][j], d[2][0][j], d[3][0][j]); s1[j] = add3(d[0][1][j], d[1][1][j], d[2][1][j], d[3][1][j]); }
      *(LAS u32x2*)(kw) = u32x2{cvt2(s0[0], s0[1]), cvt2(s0[2], s0[3])};
      *(LAS u32x2*)(kw + 640) = u32x2{cvt2(s1[0], s1[1]), cvt2(s1[2], s1[3])};
      *(LAS u32x2*)(zw) = u32x2{cvt2(dz[0], dz[1]), cvt2(dz[2], dz[3])};
    }
    STAMP(25);
    BAR();
    __builtin_amdgcn_s_setprio(1);
    STAMP(26);
    unsigned aor0 = L + L_SCR + 2048 + lane * 16, aor = L + lane * 16;
    unsigned ew0 = scr + r32 * 64 + (((2 * h) ^ ((r32 >> 1) & 3)) << 4), ew1 = ew0 ^ 16u, er = scr + (lane >> 2) * 64 + (((lane & 3) ^ ((lane >> 3) & 3)) << 4);
    asm volatile("" : "+v"(aor0), "+v"(aor), "+v"(ew0), "+v"(ew1), "+v"(er));
    RAWBAR();
    f32x16 c2[2] = {zero16(), zero16()};
    bf16x8 ob[64];
    issue_wo(IC(23), IC(32));
#pragma unroll
    for (int i = 0; i < 8; ++i) ob[i] = lds_b128(aor0 + (i & 3) * SCR + (i >> 2) * 1024);
#pragma unroll
    for (int hd = 16; hd < 23; ++hd) RW(hd);
#pragma unroll
    for (int i = 0; i < 8; ++i) c2[i >> 2] = mfma32(wf[i & 3], ob[i], c2[i >> 2]);
    SB();
    WAITV(0);
    cnt_arrive<5>(cb, one);
    cnt_wait<5, 4>(cb);
#pragma unroll
    for (int hd = 23; hd < 32; ++hd) RW(hd);
    STAMP(30);
#define RO(n) do { ob[n] = lds_b128(aor + ((n) - 8) * 1024); } while (0)
#define MO(n) do { c2[((n) >> 2) & 1] = mfma32(wf[4 * ((n) >> 3) + ((n) & 3)], ob[n], c2[((n) >> 2) & 1]); } while (0)
    RAWBAR();
    STAMP(32);
    SB();
#pragma unroll
    for (int i = 8; i < 16; ++i) RO(i);
    SB();
#pragma unroll
    for (int i = 8; i < 16; ++i) { MO(i); SB(); RO(i + 8); SB(); }
#pragma unroll
    for (int i = 16; i < 20; ++i) MO(i);
    SB();
#pragma unroll
    for (int gp = 0; gp < 2; ++gp) {
      RAWBAR();
      SB();
      const int n0 = 24 + 16 * gp;
#pragma unroll
      for (int i = 0; i < 4; ++i) { MO(n0 - 4 + i); SB(); RO(n0 + 2 * i); RO(n0 + 2 * i + 1); SB(); }
#pragma unroll
      for (int i = 0; i < 8; ++i) { MO(n0 + i); SB(); RO(n0 + 8 + i); SB(); }
      if (gp == 0) {
#pragma unroll
        for (int i = 8; i < 12; ++i) MO(n0 + i);
        SB();
      }
    }
    RAWBAR();
    STAMP(38);
    SB();
#pragma unroll
    for (int i = 0; i < 4; ++i) { MO(48 + i); SB(); RO(56 + 2 * i); RO(57 + 2 * i); SB(); }
#pragma unroll
    for (int i = 56; i < 60; ++i) MO(i);
#pragma unroll
    for (int i = 52; i < 55; ++i) MO(i);
    SB();
    auto put = [&](int t) {
      *(LAS bf16x8*)(ew0 + t * 2048) = cvt8<0>(c2[t]);
      *(LAS bf16x8*)(ew1 + t * 2048) = cvt8<8>(c2[t]);
      asm volatile("" ::: "memory");
    };
    auto sto = [&](int i, bf16x8 v) { __builtin_amdgcn_raw_buffer_store_b128(asu(v), rsY, ov, i * 32768, 16); };
    put(0);
    bf16x8 e0 = lds_b128(er), e1 = lds_b128(er + 1024);
    SB();
    MO(55);
#pragma unroll
    for (int i = 60; i < 64; ++i) MO(i);
    SB();
    sto(0, e0); sto(1, e1);
    STAMP(39);
    put(1);
    e0 = lds_b128(er + 2048); e1 = lds_b128(er + 3072);
    sto(2, e0); sto(3, e1);
  }
}

extern "C" void kernel_launch(void* const* d_in, const int* in_sizes, int n_in, void* d_out, int out_size, void* d_ws, size_t ws_size, hipStream_t stream) {
  (void)hipFuncSetAttribute((const void*)hedgehog_fused, hipFuncAttributeMaxDynamicSharedMemorySize, L_BYTES);
  hedgehog_fused<<<256, 512, L_BYTES, stream>>>((const u16*)d_in[0], (const u16*)d_in[1], (const u16*)d_in[2], (const u16*)d_in[3],
                                                (const u16*)d_in[4], (const u16*)d_in[5], (const u16*)d_in[6], (const u16*)d_in[7],
                                                (u16*)d_out, (char*)d_ws);
}
```

```cpp
#define ROTM 1
#include <hip/hip_runtime.h>
#include <hip/hip_bf16.h>
#include <type_traits>
#pragma clang diagnostic ignored "-Wint-to-pointer-cast"

typedef unsigned short u16;
typedef unsigned long long u64;
typedef __bf16 bf16x8 __attribute__((ext_vector_type(8)));
typedef __bf16 bf16x4 __attribute__((ext_vector_type(4)));
typedef __bf16 bf16x2 __attribute__((ext_vector_type(2)));
typedef float f32x16 __attribute__((ext_vector_type(16)));
typedef float f32x4 __attribute__((ext_vector_type(4)));
typedef float f32x2 __attribute__((ext_vector_type(2)));
typedef unsigned u32x4 __attribute__((ext_vector_type(4)));
typedef unsigned u32x2 __attribute__((ext_vector_type(2)));
#define LAS __attribute__((address_space(3)))
#define DI __device__ __forceinline__
typedef __amdgpu_buffer_rsrc_t rsrc_t;

#define NB 3
#define XNT 0
#define NB7 7
#define C3AT 6
#define HS2 4
#define PRETOUCH 1
#define HINT2 1
#define STAMPS 0

constexpr unsigned WS_EPOCH = 0;
constexpr unsigned WS_KVP   = 4096;
constexpr unsigned KVH      = 1408;
constexpr unsigned KVP_STR  = 4 * KVH;
constexpr unsigned WS_OCK   = 6u << 20;
constexpr unsigned OCK_STR  = 640;
constexpr unsigned WS_OF    = 2u << 20;
constexpr unsigned WS_BYTES = 7u << 20;
constexpr unsigned WS_STAMP = 8u << 20;
constexpr unsigned SLOT   = 32768;
constexpr unsigned L_SCR  = 2 * SLOT;
constexpr unsigned SCR    = 6144;
constexpr unsigned L_WO2  = 2 * SLOT + 28672;
constexpr unsigned L_PHIW = L_WO2 + 65536;
constexpr unsigned L_ZERO = L_PHIW + 2048;
constexpr unsigned L_CNT  = L_ZERO + 16;
constexpr unsigned L_TAG  = L_CNT + 32;
constexpr unsigned L_BYTES = 163840;
constexpr unsigned SPIN_CAP = 1u << 16;
constexpr unsigned OOB = 0x80000000u;
constexpr float L2E = 1.4426950408889634f;

#define WAITV(n) asm volatile("s_waitcnt vmcnt(" #n ")" ::: "memory")
#define WAITL0() asm volatile("s_waitcnt lgkmcnt(0)" ::: "memory")
#define RAWBAR() do { asm volatile("" ::: "memory"); __builtin_amdgcn_s_barrier(); asm volatile("" ::: "memory"); } while (0)
#define BAR() do { WAITL0(); __builtin_amdgcn_s_barrier(); asm volatile("" ::: "memory"); } while (0)
#define SB() __builtin_amdgcn_sched_barrier(0)
#define PIN(...) asm volatile("" : __VA_ARGS__)
#define UNLIKELY(x) __builtin_expect(!!(x), 0)
#define IC(n) std::integral_constant<int, n>{}
template <int I, int N, class F> DI void sfor(F&& f) { if constexpr (I < N) { f(std::integral_constant<int, I>{}); sfor<I + 1, N>(f); } }

template <int LI, int SI, bool NT = false> DI void dmai(unsigned lds, unsigned voff, rsrc_t rs, unsigned soff) {
  unsigned t;
  if constexpr (NT)
    asm volatile("s_add_u32 m0, %1, %5\n\ts_add_u32 %0, %4, %6\n\tbuffer_load_dwordx4 %2, %3, %0 offen nt lds"
                 : "=&s"(t) : "s"(lds), "v"(voff), "s"(rs), "s"(soff), "i"(LI), "i"(SI) : "memory", "scc");
  else
    asm volatile("s_add_u32 m0, %1, %5\n\ts_add_u32 %0, %4, %6\n\tbuffer_load_dwordx4 %2, %3, %0 offen lds"
                 : "=&s"(t) : "s"(lds), "v"(voff), "s"(rs), "s"(soff), "i"(LI), "i"(SI) : "memory", "scc");
}
template <int LI> DI void unit_load(u64& ck, unsigned lds, unsigned l16, unsigned lck, rsrc_t rs, unsigned so, unsigned sc) {
  asm volatile("s_add_u32 m0, %1, %7\n\ts_nop 0\n\t"
               "buffer_load_dwordx4 %2, %4, %5 offen sc1 lds\n\tbuffer_load_dwordx4 %2, %4, %5 offen offset:1024 sc1 lds\n\t"
               "buffer_load_dwordx4 %2, %4, %5 offen offset:2048 sc1 lds\n\tbuffer_load_dwordx4 %2, %4, %5 offen offset:3072 sc1 lds\n\t"
               "buffer_load_dwordx2 %0, %3, %4, %6 offen sc1"
               : "+v"(ck) : "s"(lds), "v"(l16), "v"(lck), "s"(rs), "s"(so), "s"(sc), "i"(LI) : "memory", "scc");
}
DI void quad_load(u32x4 (&g)[5], unsigned gv, rsrc_t rs, unsigned so) {
  asm volatile("buffer_load_dwordx4 %0, %5, %6, %7 offen sc1\n\tbuffer_load_dwordx4 %2, %5, %6, %7 offen offset:512 sc1\n\t"
               "buffer_load_dwordx4 %1, %5, %6, %7 offen offset:256 sc1\n\tbuffer_load_dwordx4 %3, %5, %6, %7 offen offset:768 sc1\n\t"
               "buffer_load_dwordx4 %4, %5, %6, %7 offen offset:1024 sc1"
               : "+v"(g[0]), "+v"(g[1]), "+v"(g[2]), "+v"(g[3]), "+v"(g[4]) : "v"(gv), "s"(rs), "s"(so) : "memory");
}
DI void z_load(u32x4& z, u64& zc, unsigned zv, unsigned zcv, rsrc_t rs, unsigned so) {
  asm volatile("buffer_load_dwordx4 %0, %2, %4, %5 offen offset:1280 sc1\n\tbuffer_load_dwordx2 %1, %3, %4, %5 offen offset:1312 sc1"
               : "+v"(z), "+v"(zc) : "v"(zv), "v"(zcv), "s"(rs), "s"(so) : "memory");
}
DI void hint_wait(u64 tag, unsigned hv, rsrc_t rs, unsigned& spins) {
  u64 f;
  asm volatile("buffer_load_dwordx2 %0, %1, %2, 0 offen sc1\n\ts_waitcnt vmcnt(0)" : "=&v"(f) : "v"(hv), "s"(rs) : "memory");
  if (__builtin_expect(__builtin_amdgcn_ballot_w64(f != tag) != 0, 0)) {
#pragma nounroll
    do { asm volatile("buffer_load_dwordx2 %0, %1, %2, 0 offen sc1\n\ts_waitcnt vmcnt(0)" : "=&v"(f) : "v"(hv), "s"(rs) : "memory"); }
    while (++spins < (1u << 16) && __builtin_amdgcn_ballot_w64(f != tag) != 0);
  }
}
template <int N> DI void waitv() { asm volatile("s_waitcnt vmcnt(%0)" :: "i"(N) : "memory"); }
template <int OFF> DI void st16o(u32x4 v, unsigned voff, rsrc_t rs, unsigned soff) {
  asm volatile("buffer_store_dwordx4 %0, %1, %2, %3 offen offset:%4 sc1\n\ts_nop 1" :: "v"(v), "v"(voff), "s"(rs), "s"(soff), "i"(OFF) : "memory");
}
static constexpr unsigned HC[16] = {0x5E2D58Du, 0x6B8B457u, 0x7AB3C19u, 0x4C6F2A3u, 0x71D8E95u, 0x59A40CFu, 0x63F7B61u, 0x4E1936Bu,
                                    0x7F4A7C1u, 0x54D2E87u, 0x6A09E67u, 0x43C6EF3u, 0x7254FF5u, 0x510E527u, 0x69B0569u, 0x5BE0CD1u};
DI u64 hash4(u32x4 v, u64 s, const unsigned* c) {
  u64 r;
  asm("v_mad_u64_u32 %0, vcc, %1, %6, %5\n\tv_mad_u64_u32 %0, vcc, %2, %7, %0\n\tv_mad_u64_u32 %0, vcc, %3, %8, %0\n\tv_mad_u64_u32 %0, vcc, %4, %9, %0"
      : "=&v"(r) : "v"(v[0]), "v"(v[1]), "v"(v[2]), "v"(v[3]), "v"(s), "s"(c[0]), "s"(c[1]), "s"(c[2]), "s"(c[3]) : "vcc");
  return r;
}
DI u64 hash4v(u32x4 v, u64 s, u32x4 c) {
  u64 r;
  asm("v_mad_u64_u32 %0, vcc, %1, %6, %5\n\tv_mad_u64_u32 %0, vcc, %2, %7, %0\n\tv_mad_u64_u32 %0, vcc, %3, %8, %0\n\tv_mad_u64_u32 %0, vcc, %4, %9, %0"
      : "=&v"(r) : "v"(v[0]), "v"(v[1]), "v"(v[2]), "v"(v[3]), "v"(s), "v"(c[0]), "v"(c[1]), "v"(c[2]), "v"(c[3]) : "vcc");
  return r;
}
DI u32x4 pair_halves(u64 c) {
  auto lo = __builtin_amdgcn_permlane32_swap((unsigned)c, (unsigned)c, false, false);
  auto hi = __builtin_amdgcn_permlane32_swap((unsigned)(c >> 32), (unsigned)(c >> 32), false, false);
  return u32x4{lo[0], hi[0], lo[1], hi[1]};
}
DI u64 mk64(unsigned lo, unsigned hi) { return ((u64)hi << 32) | lo; }
DI bool anyl(bool p) { return __builtin_amdgcn_ballot_w64(p) != 0; }

DI bf16x8 lds_b128(unsigned a) { return *(const LAS bf16x8*)a; }
DI bf16x8 lds_tr(unsigned a0, unsigned a1) {
  bf16x4 lo = __builtin_amdgcn_ds_read_tr16_b64_v4bf16((LAS bf16x4*)a0);
  bf16x4 hi = __builtin_amdgcn_ds_read_tr16_b64_v4bf16((LAS bf16x4*)a1);
  return __builtin_shufflevector(lo, hi, 0, 1, 2, 3, 4, 5, 6, 7);
}
DI f32x16 mfma32(bf16x8 a, bf16x8 b, f32x16 c) { return __builtin_amdgcn_mfma_f32_32x32x16_bf16(a, b, c, 0, 0, 0); }
DI f32x4 mfma16(bf16x8 a, bf16x8 b, f32x4 c) { return __builtin_amdgcn_mfma_f32_16x16x32_bf16(a, b, c, 0, 0, 0); }
DI bf16x8 asbf(u32x4 v) { return __builtin_bit_cast(bf16x8, v); }
DI u32x4 asu(bf16x8 v) { return __builtin_bit_cast(u32x4, v); }
template <int O> DI bf16x8 cvt8(const f32x16& c) {
  bf16x8 r;
#pragma unroll
  for (int i = 0; i < 8; ++i) r[i] = (__bf16)c[O + i];
  return r;
}
DI unsigned cvt2(float a, float b) { bf16x2 v = {(__bf16)a, (__bf16)b}; return __builtin_bit_cast(unsigned, v); }
DI f32x16 zero16() { f32x16 z;
#pragma unroll
  for (int i = 0; i < 16; ++i) z[i] = 0.f;
  return z; }
DI bf16x8 onehot(bool on, int j) {
  const unsigned v = on ? (0x3F80u << (16 * (j & 1))) : 0u;
  return asbf(u32x4{(j >> 1) == 0 ? v : 0u, (j >> 1) == 1 ? v : 0u, (j >> 1) == 2 ? v : 0u, (j >> 1) == 3 ? v : 0u});
}
DI void swap4(u32x4& a, u32x4& c) {
#pragma unroll
  for (int i = 0; i < 4; ++i) { auto r = __builtin_amdgcn_permlane32_swap(a[i], c[i], false, false); a[i] = r[0]; c[i] = r[1]; }
}

DI float max16(const f32x16& p) {
  float m = __builtin_elementwise_maximum(p[0], p[1]);
#pragma unroll
  for (int i = 2; i < 16; i += 2) m = __builtin_elementwise_maximum(__builtin_elementwise_maximum(m, p[i]), p[i + 1]);
  return m;
}
DI float minus_exact(const f32x16& p, float (&g)[16]) {
  float mn = p[0];
#pragma unroll
  for (int i = 1; i < 16; ++i) mn = __builtin_fminf(mn, p[i]);
  float s = 0.f;
#pragma unroll
  for (int i = 0; i < 16; ++i) { g[i] = __builtin_amdgcn_exp2f((mn - p[i]) * L2E); s += g[i]; }
  return s;
}
constexpr float BIG = 0x1p120f;
DI float sum16pk(const float (&e)[16]) {
  f32x2 t[8];
#pragma unroll
  for (int i = 0; i < 8; ++i) t[i] = f32x2{e[2 * i], e[2 * i + 1]};
  const f32x2 s = ((t[0] + t[1]) + (t[2] + t[3])) + ((t[4] + t[5]) + (t[6] + t[7]));
  return s.x + s.y;
}
DI void args16pk(const f32x16& p, float mx, float (&x)[16]) {
  const float a = -mx * L2E;
#pragma unroll
  for (int i = 0; i < 8; ++i) { const f32x2 v = f32x2{p[2 * i], p[2 * i + 1]} * f32x2{L2E, L2E} + f32x2{a, a}; x[2 * i] = v.x; x[2 * i + 1] = v.y; }
}
DI void scale16pk(float (&e)[16], float r) {
#pragma unroll
  for (int i = 0; i < 8; ++i) { const f32x2 v = f32x2{e[2 * i], e[2 * i + 1]} * f32x2{r, r}; e[2 * i] = v.x; e[2 * i + 1] = v.y; }
}
DI void pack16(const float (&e)[16], u32x4& a, u32x4& c) {
#pragma unroll
  for (int i = 0; i < 4; ++i) { a[i] = cvt2(e[2 * i], e[2 * i + 1]); c[i] = cvt2(e[8 + 2 * i], e[9 + 2 * i]); }
}
template <int I0, int I1> DI void exp_r(const float (&x)[16], float (&e)[16]) {
#pragma unroll
  for (int i = I0; i < I1; ++i) e[i] = __builtin_amdgcn_exp2f(x[i]);
}
template <int I0, int I1> DI void rcp_r(const float (&e)[16], float (&g)[16]) {
#pragma unroll
  for (int i = I0; i < I1; ++i) g[i] = __builtin_amdgcn_rcpf(e[i]);
}
DI void fma4(float (&x)[16], int o, const f32x16& p, float c, float a) {
  asm("v_fma_f32 %0, %4, %8, %9\n\tv_fma_f32 %1, %5, %8, %9\n\tv_fma_f32 %2, %6, %8, %9\n\tv_fma_f32 %3, %7, %8, %9"
      : "=&v"(x[o]), "=&v"(x[o + 1]), "=&v"(x[o + 2]), "=&v"(x[o + 3]) : "v"(p[o]), "v"(p[o + 1]), "v"(p[o + 2]), "v"(p[o + 3]), "s"(c), "v"(a));
}
DI float add4(float s, const float (&e)[16], int o) {
  float r;
  asm("v_add_f32_e32 %0, %1, %2\n\tv_add_f32_e32 %0, %0, %3\n\tv_add_f32_e32 %0, %0, %4\n\tv_add_f32_e32 %0, %0, %5"
      : "=&v"(r) : "v"(s), "v"(e[o]), "v"(e[o + 1]), "v"(e[o + 2]), "v"(e[o + 3]));
  return r;
}
DI float add3(float a, float b, float c, float d) {
  float r;
  asm("v_add_f32_e32 %0, %1, %2\n\tv_add_f32_e32 %0, %0, %3\n\tv_add_f32_e32 %0, %0, %4" : "=&v"(r) : "v"(a), "v"(b), "v"(c), "v"(d));
  return r;
}
DI u32x2 mulcvt4(float a, float b, float c, float d, float r) {
  unsigned x, y; float t0, t1;
  asm("v_mul_f32_e32 %0, %4, %8\n\tv_mul_f32_e32 %2, %5, %8\n\tv_mul_f32_e32 %1, %6, %8\n\tv_mul_f32_e32 %3, %7, %8\n\t"
      "v_cvt_pk_bf16_f32 %0, %0, %2\n\tv_cvt_pk_bf16_f32 %1, %1, %3"
      : "=&v"(x), "=&v"(y), "=&v"(t0), "=&v"(t1) : "v"(a), "v"(b), "v"(c), "v"(d), "v"(r));
  return u32x2{x, y};
}

DI u32x4 mulcvt8(const f32x16& n, float r) {
  u32x4 o; float t0, t1, t2, t3;
  asm("v_mul_f32_e32 %0, %8, %16\n\tv_mul_f32_e32 %4, %9, %16\n\tv_mul_f32_e32 %1, %10, %16\n\tv_mul_f32_e32 %5, %11, %16\n\t"
      "v_mul_f32_e32 %2, %12, %16\n\tv_mul_f32_e32 %6, %13, %16\n\tv_mul_f32_e32 %3, %14, %16\n\tv_mul_f32_e32 %7, %15, %16\n\t"
      "v_cvt_pk_bf16_f32 %0, %0, %4\n\tv_cvt_pk_bf16_f32 %1, %1, %5\n\tv_cvt_pk_bf16_f32 %2, %2, %6\n\tv_cvt_pk_bf16_f32 %3, %3, %7"
      : "=&v"(o[0]), "=&v"(o[1]), "=&v"(o[2]), "=&v"(o[3]), "=&v"(t0), "=&v"(t1), "=&v"(t2), "=&v"(t3)
      : "v"(n[0]), "v"(n[1]), "v"(n[2]), "v"(n[3]), "v"(n[4]), "v"(n[5]), "v"(n[6]), "v"(n[7]), "v"(r));
  return o;
}

#define STAMP(i) do {} while (0)
#define STAMPR(i) do {} while (0)
#define STAMPV(i, x) do {} while (0)

template <int C> DI void cnt_arrive(unsigned base, unsigned one) {
  asm volatile("s_mov_b64 exec, 1\n\tds_add_u32 %0, %1 offset:%2\n\ts_mov_b64 exec, -1" :: "v"(base), "v"(one), "i"(4 * C) : "memory");
}
template <int C, int N> DI void cnt_wait(unsigned base) {
  unsigned v, t;
  asm volatile("s_branch 2f\n1:\n\ts_sleep 1\n2:\n\tds_read_b32 %0, %2 offset:%3\n\ts_waitcnt lgkmcnt(0)\n\tv_readfirstlane_b32 %1, %0\n\ts_nop 0\n\ts_cmp_lt_u32 %1, %4\n\ts_cbranch_scc1 1b"
               : "=&v"(v), "=&s"(t) : "v"(base), "i"(4 * C), "i"(N) : "memory", "scc");
}

__global__ __launch_bounds__(512, 2) void hedgehog_fused(
    const u16* __restrict__ xA, const u16* __restrict__ wA, const u16* __restrict__ pA, const u16* __restrict__ oA,
    const u16* __restrict__ xB, const u16* __restrict__ wB, const u16* __restrict__ pB, const u16* __restrict__ oB,
    u16* __restrict__ out, char* __restrict__ ws) {
  extern __shared__ __attribute__((aligned(16))) char smem[];
  STAMPR(60); STAMP(0);
  const unsigned L = (unsigned)(size_t)(LAS char*)smem;
  const int b = blockIdx.x, xcd = b & 7, jj = b >> 3;
  const int br = xcd >> 2, rt = (xcd & 3) * 4 + (jj >> 3), hg = jj & 7;
  const int rot = ROTM == 0 ? 2 * (xcd & 3) : ROTM == 5 ? (hg + 2 * (rt & 3)) % 7 : ROTM == 6 ? (hg + 2 * (rt & 3)) % 6 : (hg + 2 * (rt & 3)) & 7;
  auto chunk_of = [&](int v) -> unsigned {
    if (ROTM == 5) { const int t = v - 1 + rot; return (unsigned)((v == 0 ? 0 : 1 + (t >= 7 ? t - 7 : t)) + 2 * (xcd & 3)) & 7; }
    if (ROTM == 6) { const int t = v - 2 + rot; return (unsigned)((v < 2 ? v : 2 + (t >= 6 ? t - 6 : t)) + 2 * (xcd & 3)) & 7; }
    return (unsigned)((v + rot) & 7);
  };
  const int tid = threadIdx.x, lane = tid & 63;
  const int w = __builtin_amdgcn_readfirstlane(tid >> 6);
  const int u = w & 3;
  const int r32 = lane & 31, h = lane >> 5, blk = (lane >> 4) & 1, q4 = (lane & 15) >> 2, p4 = lane & 3;
  const unsigned fx = (unsigned)(h ^ ((r32 >> 1) & 7));
#define MAKE_WO() \
  rsrc_t rsO = __builtin_amdgcn_make_buffer_rsrc((void*)(br ? oB : oA), 0, 512 * 1024, 0x00020000); \
  rsrc_t rsS = __builtin_amdgcn_make_buffer_rsrc((void*)ws, 0, WS_BYTES, 0x00020000); \
  PIN("+s"(rsO), "+s"(rsS)); \
  unsigned vwo = (unsigned)((lane >> 4) * 1024 + (hg & 3) * 256 + (((lane & 15) ^ ((lane >> 4) << 2)) << 4)); \
  unsigned cb = L + L_CNT, one = 1; asm volatile("" : "+v"(cb), "+v"(one), "+v"(vwo)); \
  unsigned lwo = L + u * 1024; PIN("+s"(lwo)); \
  auto issue_wo = [&](auto F0, auto F1) {     \
    sfor<decltype(F0)::value, decltype(F1)::value>([&](auto F) { constexpr int f = decltype(F)::value; \
      dmai<WO_AT(f), (f & 3) * 16384>(lwo, vwo, rsO, (unsigned)(((hg + (f >> 2)) & 7) * 65536 + u * 4096)); }); \
  };
#define WO_AT(f) ((f) < 16 ? 2 * SLOT + (f) * 4096 : ((f) < 23 ? L_WO2 + ((f) - 7) * 4096 : L_WO2 + ((f) - 23) * 4096))

  if (__builtin_expect(w >= 4, 1)) {
    const rsrc_t rsX = __builtin_amdgcn_make_buffer_rsrc((void*)((br ? xB : xA) + (size_t)rt * 128 * 512), 0, 128 * 1024, 0x00020000);
    const rsrc_t rsW = __builtin_amdgcn_make_buffer_rsrc((void*)(br ? wB : wA), 0, 512 * 3072, 0x00020000);
    const unsigned vxe = (unsigned)((lane >> 3) * 1024 + (((lane & 7) ^ (lane >> 4)) << 4));
    const unsigned vxo = vxe ^ 64u;
    const unsigned vwk = (unsigned)((lane >> 3) * 3072 + hg * 128 + (((lane & 7) ^ (((lane >> 4) & 1) << 2)) << 4));
    const unsigned vwv = vwk ^ 32u;
    const unsigned lx = L + u * 4096, lw = L + u * 2048;
    auto issue = [&](auto V, auto I0, auto I1) {
      constexpr int v = decltype(V)::value;
      if constexpr (v < 8) {
        const unsigned c = chunk_of(v);
        const unsigned sx = c * 128 + u * 32768, sw = c * (64 * 3072) + u * (16 * 3072);
        sfor<decltype(I0)::value, decltype(I1)::value>([&](auto I) {
          constexpr int i = decltype(I)::value;
          if constexpr (i < 4) dmai<(v & 3) * SLOT + i * 1024, i * 8192, (v < XNT)>(lx, (i & 1) ? vxo : vxe, rsX, sx);
          else if constexpr (i < 6) dmai<(v & 3) * SLOT + 16384 + (i - 4) * 1024, (i - 4) * (8 * 3072) + 1024>(lw, vwk, rsW, sw);
          else dmai<(v & 3) * SLOT + 24576 + (i - 6) * 1024, (i - 6) * (8 * 3072) + 2048>(lw, vwv, rsW, sw);
        });
      } else {
        sfor<decltype(I0)::value, decltype(I1)::value>([&](auto I) {
          constexpr int i = decltype(I)::value;
          const unsigned c = chunk_of(4 * (v - 8) + (i >> 1));
          dmai<(v - 8) * SLOT + (i >> 1) * 8192 + (i & 1) * 1024, (i & 1) * (8 * 3072)>(lw, vwk, rsW, c * (64 * 3072) + u * (16 * 3072));
        });
      }
    };
    asm volatile("s_nop 4" ::: "memory");
    STAMP(50);
    issue(IC(0), IC(0), IC(8)); issue(IC(1), IC(0), IC(8));
    MAKE_WO();
    u64 tag;
    bf16x8 xf[32], ap[4];
    unsigned axk[2][4];
#pragma unroll
    for (int ks = 0; ks < 4; ++ks) { axk[0][ks] = L + (32 * u + r32) * 128 + ((fx ^ (2 * ks)) << 4); axk[1][ks] = axk[0][ks] + 2 * SLOT; asm volatile("" : "+v"(axk[1][ks])); }
    unsigned aw0 = L + (8 * h + q4) * 128 + (((2 * blk + (p4 & 1)) ^ ((q4 >> 1) << 2)) << 4) + 8 * (p4 >> 1), aw1 = aw0 ^ 64u;
    unsigned apb = L + L_PHIW + (8 * h + q4) * 32 + (2 * blk + (p4 >> 1)) * 8;
    asm volatile("" : "+v"(aw0), "+v"(aw1), "+v"(apb));
    sfor<0, 8>([&](auto S) {
      constexpr int s = decltype(S)::value;
      if constexpr (s < 7) { issue(IC(s + 2), IC(0), IC(NB)); waitv<8 + NB>(); }
      else waitv<NB7>();
      BAR();
      if (s == 0) { STAMP(2); STAMPR(52); } if (s == 7) { STAMP(9); STAMPR(53); }
      if constexpr (s < 6) issue(IC(s + 2), IC(NB), IC(8));
      if constexpr (s == 6) issue(IC(8), IC(NB), IC(NB7));
      if constexpr (s == 7) { issue(IC(8), IC(NB7), IC(8)); issue(IC(9), IC(0), IC(5)); }
      if (s == 1) tag = *(const LAS u64*)(L + L_TAG);
      if constexpr (s >= 2 && s < 6) {
        const bool on = (p4 & 1) == (s & 1);
        ap[s - 2] = lds_tr(on ? apb + (s - 2) * 512 : L + L_ZERO, on ? apb + (s - 2) * 512 + 128 : L + L_ZERO);
      }
#pragma unroll
      for (int ks = 0; ks < 4; ++ks) xf[4 * s + ks] = lds_b128(axk[(s >> 1) & 1][ks] + (s & 1) * SLOT);
      SB();
    });
    cnt_arrive<0>(cb, one);
    issue(IC(9), IC(5), IC(8));
    issue_wo(IC(0), IC(8));
    cnt_wait<0, 8>(cb);
    issue_wo(IC(8), IC(16));
    STAMP(10);
    waitv<16>();
    cnt_arrive<1>(cb, one);
    unsigned hc[16];
#pragma unroll
    for (int i = 0; i < 16; ++i) { hc[i] = HC[i]; asm volatile("" : "+s"(hc[i])); }
    unsigned kb = L + L_SCR + h * 320 + (r32 < 16 ? r32 : 16 + (r32 & 3)) * 16;
    unsigned aown = L + L_SCR + 2048 + (u & 1) * 1024 + lane * 16;
    unsigned l16 = lane * 16, l16h = lane < 40 ? l16 : OOB, lck = r32 * 16 + h * 8, avf = lx + lane * 16;
    asm volatile("" : "+v"(kb), "+v"(aown), "+v"(l16), "+v"(l16h), "+v"(lck), "+v"(avf));
    unsigned gso[4], gsc[4], hv;
    u64 pre = 0;
    {
      const unsigned tt2 = (unsigned)(br * 64 + 4 * rt + 2 * (hg >> 2) + (u & 1));
#pragma unroll
      for (int g = 0; g < 4; ++g) {
        const unsigned pr = (unsigned)(hg + 2 * g + 1 + (u >> 1)) & 7;
        gso[g] = WS_OF + (tt2 * 32 + 4 * pr) * 1024; gsc[g] = WS_OCK + (tt2 * 8 + pr) * OCK_STR;
        asm volatile("" : "+s"(gso[g]), "+s"(gsc[g]));
      }
      hv = WS_OCK + (tt2 * 8 + ((unsigned)(hg + 2 * (u < 2 ? lane & 3 : lane % 3u) + 1 + (u >> 1)) & 7)) * OCK_STR + 512;
      PIN("+v"(hv));
    }
    unsigned of_own = WS_OF + (unsigned)(((br * 64 + 4 * rt + u) * 32 + 4 * hg) * 1024), ock = WS_OCK + (unsigned)((br * 64 + 4 * rt + u) * 8 + hg) * OCK_STR;
    asm volatile("" : "+s"(of_own), "+s"(ock));
    { u64 f; asm volatile("buffer_load_dwordx2 %0, %1, %2, 0 offen sc1" : "=v"(f) : "v"(hv), "s"(rsS) : "memory"); pre = f; }
    STAMP(11);
    cnt_wait<1, 4>(cb);
    bf16x8 wq[4];
#define RQ(n) wq[(n) & 3] = lds_tr(((n) < 32 ? aw0 : aw1) + ((n) & 31) * 2048, ((n) < 32 ? aw0 : aw1) + ((n) & 31) * 2048 + 512)
    RQ(0); RQ(1); RQ(2);
    cnt_wait<2, 4>(cb);
    __builtin_amdgcn_s_setprio(1);
    STAMP(12);
    f32x16 q0 = zero16(), q1 = zero16();
    SB();
#pragma unroll
    for (int n = 0; n < 32; ++n) {
      RQ(n + 3); q0 = mfma32(wq[n & 3], xf[n], q0); SB();
      if (n == C3AT) { waitv<PRETOUCH>(); cnt_arrive<3>(cb, one); SB(); }
    }
    u32x4 fq[4][2];
    {
      f32x16 p;
      float x[16], e[16], g[16], ca, se, sg, re, rg;
      bf16x8 c0, c1;
      sfor<32, 64>([&](auto N) {
        constexpr int n = decltype(N)::value, i = n - 32;
        if constexpr (n + 3 < 64) RQ(n + 3);
        q1 = mfma32(wq[n & 3], xf[i], q1);
        SB();
        if constexpr (i == 2) c0 = cvt8<0>(q0);
        if constexpr (i == 3) { c1 = cvt8<8>(q0); p = mfma32(ap[0], c0, zero16()); }
        if constexpr (i == 4) p = mfma32(ap[1], c1, p);
        if constexpr (i == 8) ca = -max16(p) * L2E;
        if constexpr (i == 9) { fma4(x, 0, p, L2E, ca); fma4(x, 4, p, L2E, ca); }
        if constexpr (i == 10) { exp_r<0, 3>(x, e); }
        if constexpr (i == 11) { fma4(x, 8, p, L2E, ca); exp_r<3, 5>(x, e); }
        if constexpr (i == 12) { fma4(x, 12, p, L2E, ca); exp_r<5, 7>(x, e); }
        if constexpr (i >= 13 && i < 16) exp_r<7 + 3 * (i - 13), 10 + 3 * (i - 13)>(x, e);
        if constexpr (i == 16) { rcp_r<0, 2>(e, g); se = add3(e[0], e[1], e[2], e[3]); }
        if constexpr (i >= 17 && i < 20) { rcp_r<2 * (i - 16), 2 * (i - 15)>(e, g); se = add4(se, e, 4 * (i - 16)); }
        if constexpr (i == 20) { re = __builtin_amdgcn_rcpf(se); PIN("+v"(re)); rcp_r<8, 10>(e, g); sg = add3(g[0], g[1], g[2], g[3]); }
        if constexpr (i == 21) { rcp_r<10, 12>(e, g); sg = add4(sg, g, 4); }
        if constexpr (i == 22) { rcp_r<12, 16>(e, g); }
        if constexpr (i == 23) { sg = add4(sg, g, 8); sg = add4(sg, g, 12); }
        if constexpr (i == 24) { if (UNLIKELY(anyl(!(sg < BIG)))) sg = minus_exact(p, g); rg = __builtin_amdgcn_rcpf(sg); }
        if constexpr (i >= 24 && i < 28) { constexpr int k = i - 24; const u32x2 t = mulcvt4(e[4 * k], e[4 * k + 1], e[4 * k + 2], e[4 * k + 3], re); fq[k >> 1][0][2 * (k & 1)] = t.x; fq[k >> 1][0][2 * (k & 1) + 1] = t.y; }
        if constexpr (i >= 28) { constexpr int k = i - 28; const u32x2 t = mulcvt4(g[4 * k], g[4 * k + 1], g[4 * k + 2], g[4 * k + 3], rg); fq[k >> 1][1][2 * (k & 1)] = t.x; fq[k >> 1][1][2 * (k & 1) + 1] = t.y; }
        SB();
      });
      STAMP(13);
      asm volatile("s_nop 1" : "+v"(fq[0][0]), "+v"(fq[1][0]), "+v"(fq[0][1]), "+v"(fq[1][1]));
      swap4(fq[0][0], fq[1][0]);
      SB();
      c0 = cvt8<0>(q1); c1 = cvt8<8>(q1);
      p = mfma32(ap[2], c0, zero16());
      p = mfma32(ap[3], c1, p);
      SB();
      swap4(fq[0][1], fq[1][1]);
      SB();
      args16pk(p, max16(p), x);
      exp_r<0, 16>(x, e); rcp_r<0, 16>(e, g);
      se = sum16pk(e); sg = sum16pk(g); PIN("+v"(se));
      if (UNLIKELY(anyl(!(sg < BIG)))) sg = minus_exact(p, g);
      scale16pk(e, __builtin_amdgcn_rcpf(se)); scale16pk(g, __builtin_amdgcn_rcpf(sg));
      pack16(e, fq[2][0], fq[3][0]); pack16(g, fq[2][1], fq[3][1]);
      swap4(fq[2][0], fq[3][0]); swap4(fq[2][1], fq[3][1]);
    }
#pragma unroll
    for (int a = 0; a < 4; ++a) asm volatile("" : "+v"(fq[a][0]), "+v"(fq[a][1]));
    __builtin_amdgcn_s_setprio(3);
    WAITV(0);
    asm volatile("" :: "v"(pre));
    STAMP(14);
    RAWBAR();
    STAMP(26); STAMPR(55);
    {
      bf16x8 ka[4][2];
#pragma unroll
      for (int a = 0; a < 4; ++a) { ka[a][0] = lds_b128(kb + a * SCR); ka[a][1] = lds_b128(kb + a * SCR + 640); }
      SB();
      f32x16 n[4];
      u32x4 ou[4];
      u64 hs = tag;
      auto mm = [&](int a) { n[a] = mfma32(ka[a][0], asbf(fq[a][0]), zero16()); n[a] = mfma32(ka[a][1], asbf(fq[a][1]), n[a]); SB(); };
      float rd[4];
      auto den = [&](int a) { rd[a] = __builtin_amdgcn_rcpf((((n[a][8] + n[a][9]) + n[a][10]) + n[a][11]) + 1e-6f); SB(); };
      auto fin = [&](auto A) { constexpr int a = decltype(A)::value; ou[a] = mulcvt8(n[a], rd[a]); st16o<a * 1024>(ou[a], l16, rsS, of_own); SB(); };
      mm(0); mm(1); mm(2);
      den(0);
      mm(3);
      fin(IC(0)); den(1); hs = hash4(ou[0], hs, hc); SB();
      fin(IC(1)); den(2); hs = hash4(ou[1], hs, hc + 4); SB();
      fin(IC(2)); den(3); hs = hash4(ou[2], hs, hc + 8); SB();
      fin(IC(3)); hs = hash4(ou[3], hs, hc + 12);
      asm volatile("" :: "v"(n[0]), "v"(n[1]), "v"(n[2]), "v"(n[3]));
      u32x4 cw = pair_halves(hs);
      cw[0] = h ? (unsigned)tag : cw[0]; cw[1] = h ? (unsigned)(tag >> 32) : cw[1];
      st16o<0>(cw, l16h, rsS, ock);
      if ((u >> 1) == (hg >> 2)) {
#pragma unroll
        for (int a = 0; a < 4; ++a) *(LAS u32x4*)(aown + a * SCR) = ou[a];
      }
    }
    STAMP(27);
    BAR();
    unsigned spins2 = 0;
    __builtin_amdgcn_s_sleep(HS2);
    STAMP(28);
    hint_wait(tag, hv, rsS, spins2);
    STAMP(29);
    u64 ck[4];
    asm volatile("" : "=v"(ck[0]), "=v"(ck[1]), "=v"(ck[2]), "=v"(ck[3]));
    auto verify = [&](auto G) {
      constexpr int g = decltype(G)::value;
      auto bad = [&]() {
        asm volatile("" : "+v"(ck[g]) :: "memory");
        const u32x4 v0 = *(const LAS u32x4*)(avf + g * 16384), v1 = *(const LAS u32x4*)(avf + g * 16384 + 1024);
        const u32x4 v2 = *(const LAS u32x4*)(avf + g * 16384 + 2048), v3 = *(const LAS u32x4*)(avf + g * 16384 + 3072);
        return anyl(hash4(v3, hash4(v2, hash4(v1, hash4(v0, tag, hc), hc + 4), hc + 8), hc + 12) != ck[g]);
      };
      if (UNLIKELY(bad())) {
#pragma nounroll
        do { unit_load<g * 16384>(ck[g], lx, l16, lck, rsS, gso[g], gsc[g]); WAITV(0); } while (++spins2 < SPIN_CAP && bad());
      }
    };
    unit_load<0>(ck[0], lx, l16, lck, rsS, gso[0], gsc[0]);
    unit_load<16384>(ck[1], lx, l16, lck, rsS, gso[1], gsc[1]);
    STAMP(30);
    waitv<5>();
    STAMP(31);
    unit_load<32768>(ck[2], lx, l16, lck, rsS, gso[2], gsc[2]);
    verify(IC(0));
    RAWBAR();
    STAMP(32); STAMPR(56);
    if (u < 2) unit_load<49152>(ck[3], lx, l16, lck, rsS, gso[3], gsc[3]);
    if (u < 2) waitv<10>(); else waitv<5>();
    verify(IC(1));
    STAMP(33);
    RAWBAR();
    if (u < 2) waitv<5>(); else waitv<0>();
    verify(IC(2));
    STAMP(34);
    RAWBAR();
    if (u < 2) { waitv<0>(); verify(IC(3)); }
    STAMP(35);
    RAWBAR();
    STAMP(38);
    STAMPV(63, spins2);
    if (rt == 0 && hg == 0 && u == 0)
      asm volatile("buffer_store_dwordx2 %0, %1, %2, %3 offen sc1" :: "v"(tag), "v"(lane == 0 ? 0u : OOB), "s"(rsS), "s"(WS_EPOCH + br * 128) : "memory");
  } else {
    {
      const rsrc_t rsP = __builtin_amdgcn_make_buffer_rsrc((void*)(br ? pB : pA), 0, 32 * 512, 0x00020000);
      asm volatile("s_nop 4" ::: "memory");
      if (u < 2) dmai<L_PHIW, 0>(L + u * 1024, (unsigned)(lane * 16), rsP, (unsigned)(hg * 2048 + u * 1024));
      if (u == 3) {
        u64 e;
        asm volatile("s_load_dwordx2 %0, %1, 0x0\n\ts_waitcnt lgkmcnt(0)" : "=&s"(e) : "s"(ws + WS_EPOCH + br * 128) : "memory");
        if (lane < 3) *(LAS u32x4*)(L + L_ZERO + lane * 16) = u32x4{0, 0, 0, 0};
        if (lane == 3) *(LAS u64*)(L + L_TAG) = e + 1;
      }
    }
    MAKE_WO();
    f32x16 acc[4] = {zero16(), zero16(), zero16(), zero16()};
    unsigned axk[2][4];
#pragma unroll
    for (int ks = 0; ks < 4; ++ks) { axk[0][ks] = L + r32 * 128 + ((fx ^ (2 * ks)) << 4); axk[1][ks] = axk[0][ks] + 2 * SLOT; asm volatile("" : "+v"(axk[0][ks]), "+v"(axk[1][ks])); }
    unsigned awb[2];
    awb[0] = L + 16384 + blk * 8192 + (8 * h + q4) * 128 + (((2 * u + (p4 & 1)) ^ ((q4 >> 1) << 2) ^ (blk << 1)) << 4) + 8 * (p4 >> 1);
    awb[1] = awb[0] + 2 * SLOT; asm volatile("" : "+v"(awb[0]), "+v"(awb[1]));
    const unsigned scr = L + L_SCR + u * SCR;
    const int jt = (lane & 3) + 4 * ((lane >> 3) & 1);
    unsigned so0, so2, apa[2][2];
    {
      const unsigned dcol = 8 * ((lane >> 2) & 1) + jt;
      so0 = blk == 0 ? h * 256 + dcol * 16 : OOB;
      so2 = blk == 0 ? (h ? OOB : 1024 + dcol * 16) : ((lane & 15) < 4 ? 1280u + h * 64 + p4 * 16 : OOB);
      asm volatile("" : "+v"(so0), "+v"(so2));
#pragma unroll
      for (int s = 0; s < 2; ++s) {
        const unsigned base = L + L_PHIW + u * 512 + (8 * h + q4) * 32 + (2 * blk + (p4 >> 1)) * 8;
        const bool on = (p4 & 1) == s;
        apa[s][0] = on ? base : L + L_ZERO; apa[s][1] = on ? base + 128 : L + L_ZERO;
        asm volatile("" : "+v"(apa[s][0]), "+v"(apa[s][1]));
      }
    }
    bf16x8 tsel[2] = {onehot(((lane >> 2) & 1) == h && blk == 0, jt), onehot(((lane >> 2) & 1) == h && blk == 1, jt)};
    bf16x8 sel[2] = {onehot(((lane >> 3) & 1) == 0, lane & 7), onehot(((lane >> 3) & 1) == 1, lane & 7)};
    f32x16 cone;
#pragma unroll
    for (int i = 0; i < 16; ++i) cone[i] = blk ? 1.f : 0.f;
    asm volatile("" : "+v"(tsel[0]), "+v"(tsel[1]), "+v"(sel[0]), "+v"(sel[1]), "+v"(cone));
    unsigned awo = L + 2 * SLOT + (4 * h + q4) * 256 + (((4 * u + 2 * (p4 & 1) + blk) ^ (q4 << 2)) << 4) + 8 * (p4 >> 1);
    unsigned awo2 = awo + (L_WO2 - 2 * SLOT); PIN("+v"(awo2));
    unsigned gv = (unsigned)((lane >> 4) * KVP_STR + (lane & 15) * 16);
    unsigned zv = (unsigned)((lane >> 2) * KVP_STR + ((lane >> 1) & 1) * 64 + (lane & 1) * 16), zcv = zv - (lane & 1) * 8;
    unsigned kw = scr + h * 320 + (lane & 15) * 16 + blk * 8;
    unsigned zw = h ? scr + 1280 + lane * 8 : scr + (2 * (lane & 1) + ((lane >> 1) & 1)) * 320 + 256 + ((lane >> 2) & 3) * 16 + blk * 8;
    unsigned ov = (unsigned)((lane >> 2) * 2048 + (lane & 3) * 16);
    rsrc_t rsY = __builtin_amdgcn_make_buffer_rsrc((void*)(out + (size_t)(rt * 128 + (hg >> 2) * 64) * 1024 + br * 512 + (hg & 3) * 128 + u * 32), 0, 63 * 2048 + 64, 0x00020000);
    asm volatile("" : "+v"(awo), "+v"(gv), "+v"(zv), "+v"(zcv), "+v"(kw), "+v"(zw), "+v"(ov), "+s"(rsY));
    unsigned kvp_grp = WS_KVP + (unsigned)((br * 8 + hg) * 16) * KVP_STR + u * KVH, kvp_own = kvp_grp + rt * KVP_STR;
    unsigned qso[4], hc[8];
#pragma unroll
    for (int i = 0; i < 4; ++i) { qso[i] = kvp_grp + (unsigned)(((rt >> 2) + i) & 3) * (4 * KVP_STR); asm volatile("" : "+s"(qso[i])); }
#pragma unroll
    for (int i = 0; i < 8; ++i) { hc[i] = HC[i]; asm volatile("" : "+s"(hc[i])); }
    u32x4 zhc;
#pragma unroll
    for (int i = 0; i < 4; ++i) zhc[i] = HC[i] + (unsigned)(lane & 1) * (HC[4 + i] - HC[i]);
    asm volatile("" : "+s"(kvp_grp), "+s"(kvp_own), "+v"(zhc));
    u64 tag;
    bf16x8 fa[3], fb[3][4], ap[2];
    WAITV(0);
    STAMP(1);
#pragma unroll
    for (int s = 0; s < 8; ++s) {
      BAR();
      if (s == 0) STAMP(2); if (s == 7) STAMP(9);
      const unsigned sl = (s & 1) * SLOT;
#define RD_A(ks) fa[(4 * s + (ks)) % 3] = lds_tr(awb[(s >> 1) & 1] + sl + (ks) * 2048, awb[(s >> 1) & 1] + sl + (ks) * 2048 + 512)
#define RD_B(ks, t) fb[(4 * s + (ks)) % 3][t] = lds_b128(axk[(s >> 1) & 1][ks] + sl + (t) * 4096)
#define MM(n, t) acc[t] = mfma32(fa[(n) % 3], fb[(n) % 3][t], acc[t])
      SB();
      if (s > 0) {
        MM(4 * s - 1, 0); SB(); RD_A(0); RD_B(0, 0); SB();
        MM(4 * s - 1, 1); SB(); RD_B(0, 1); RD_B(0, 2); RD_B(0, 3); SB();
        MM(4 * s - 1, 2); SB(); RD_A(1); RD_B(1, 0); SB();
        MM(4 * s - 1, 3); SB(); RD_B(1, 1); RD_B(1, 2); RD_B(1, 3); SB();
      } else {
        RD_A(0); RD_B(0, 0); RD_B(0, 1); RD_B(0, 2); RD_B(0, 3); RD_A(1); RD_B(1, 0); RD_B(1, 1); RD_B(1, 2); RD_B(1, 3); SB();
      }
      MM(4 * s, 0); SB(); RD_A(2); SB();
      MM(4 * s, 1); SB(); RD_B(2, 0); RD_B(2, 1); SB();
      MM(4 * s, 2); SB(); RD_B(2, 2); RD_B(2, 3); SB();
      MM(4 * s, 3); SB();
      MM(4 * s + 1, 0); SB(); RD_A(3); SB();
      MM(4 * s + 1, 1); SB(); RD_B(3, 0); RD_B(3, 1); SB();
      MM(4 * s + 1, 2); SB(); RD_B(3, 2); RD_B(3, 3); SB();
      MM(4 * s + 1, 3); SB();
      if (s == 7) { cnt_arrive<0>(cb, one); SB(); }
      if (s == 1) { ap[0] = lds_tr(apa[0][0], apa[0][1]); ap[1] = lds_tr(apa[1][0], apa[1][1]); tag = *(const LAS u64*)(L + L_TAG); SB(); }
      if (s < 7) { MM(4 * s + 2, 0); MM(4 * s + 2, 1); MM(4 * s + 2, 2); MM(4 * s + 2, 3); SB(); }
    }
    __builtin_amdgcn_s_setprio(2);
    f32x16 y;
    {
      bf16x8 kc[4], vc[4], vb[4][2];
      f32x16 p0, p1, tv[4], tp[2];
      float x[16], e[16], g[16], x1[16], e1[16], g1[16];
      u32x4 fp[2], fm[2];
      MM(30, 0); MM(31, 0); MM(30, 1); MM(31, 1); MM(30, 2); SB();
      kc[0] = cvt8<0>(acc[0]); SB(); MM(31, 2); SB();
      vc[0] = cvt8<8>(acc[0]); SB(); MM(30, 3); SB();
      kc[1] = cvt8<0>(acc[1]); SB(); MM(31, 3); SB();
      vc[1] = cvt8<8>(acc[1]); SB();
      p0 = mfma32(ap[0], kc[0], zero16()); SB(); kc[2] = cvt8<0>(acc[2]); SB();
      p0 = mfma32(ap[1], kc[1], p0); SB(); vc[2] = cvt8<8>(acc[2]); SB();
      tv[0] = mfma32(vc[0], tsel[0], cone); SB(); kc[3] = cvt8<0>(acc[3]); SB();
      tv[1] = mfma32(vc[1], tsel[0], cone); SB(); vc[3] = cvt8<8>(acc[3]); SB();
      p1 = mfma32(ap[0], kc[2], zero16()); SB();
      const float m0 = max16(p0); SB();
      p1 = mfma32(ap[1], kc[3], p1); SB(); vb[0][0] = cvt8<0>(tv[0]); vb[0][1] = cvt8<8>(tv[0]); PIN("+v"(vb[0][0]), "+v"(vb[0][1])); SB();
      tv[2] = mfma32(vc[2], tsel[0], cone); SB(); vb[1][0] = cvt8<0>(tv[1]); vb[1][1] = cvt8<8>(tv[1]); PIN("+v"(vb[1][0]), "+v"(vb[1][1])); SB();
      tv[3] = mfma32(vc[3], tsel[0], cone); SB();
      float m1 = max16(p1); PIN("+v"(m1)); SB();
      vb[2][0] = cvt8<0>(tv[2]); vb[2][1] = cvt8<8>(tv[2]); PIN("+v"(vb[2][0]), "+v"(vb[2][1])); SB();
      args16pk(p0, m0, x); args16pk(p1, m1, x1); SB();
      exp_r<0, 16>(x, e); vb[3][0] = cvt8<0>(tv[3]); vb[3][1] = cvt8<8>(tv[3]); PIN("+v"(vb[3][0]), "+v"(vb[3][1])); rcp_r<0, 16>(e, g); SB();
      float se = sum16pk(e), sg = sum16pk(g); PIN("+v"(se));
      if (UNLIKELY(anyl(!(sg < BIG)))) sg = minus_exact(p0, g);
      scale16pk(e, __builtin_amdgcn_rcpf(se)); scale16pk(g, __builtin_amdgcn_rcpf(sg)); SB();
      STAMP(15);
      pack16(e, fp[0], fp[1]); swap4(fp[0], fp[1]); SB();
      tp[0] = mfma32(asbf(fp[0]), tsel[0], zero16()); SB(); pack16(g, fm[0], fm[1]); SB();
      tp[1] = mfma32(asbf(fp[1]), tsel[0], zero16()); SB(); swap4(fm[0], fm[1]); SB();
      tp[0] = mfma32(asbf(fm[0]), tsel[1], tp[0]); SB(); exp_r<0, 4>(x1, e1); SB();
      tp[1] = mfma32(asbf(fm[1]), tsel[1], tp[1]); SB(); exp_r<4, 16>(x1, e1); SB();
      kc[0] = cvt8<0>(tp[0]); kc[1] = cvt8<8>(tp[0]); SB();
      y = mfma32(kc[0], vb[0][0], zero16()); SB(); kc[2] = cvt8<0>(tp[1]); kc[3] = cvt8<8>(tp[1]); SB();
      y = mfma32(kc[1], vb[0][1], y); SB(); rcp_r<0, 4>(e1, g1); SB();
      y = mfma32(kc[2], vb[1][0], y); SB(); rcp_r<4, 8>(e1, g1); SB();
      y = mfma32(kc[3], vb[1][1], y); SB(); rcp_r<8, 16>(e1, g1); SB();
      se = sum16pk(e1); sg = sum16pk(g1); PIN("+v"(se));
      if (UNLIKELY(anyl(!(sg < BIG)))) sg = minus_exact(p1, g1);
      scale16pk(e1, __builtin_amdgcn_rcpf(se)); scale16pk(g1, __builtin_amdgcn_rcpf(sg)); SB();
      cnt_arrive<2>(cb, one); SB();
      pack16(e1, fp[0], fp[1]); swap4(fp[0], fp[1]); SB();
      tp[0] = mfma32(asbf(fp[0]), tsel[0], zero16()); SB(); pack16(g1, fm[0], fm[1]); SB();
      tp[1] = mfma32(asbf(fp[1]), tsel[0], zero16()); SB(); swap4(fm[0], fm[1]); SB();
      tp[0] = mfma32(asbf(fm[0]), tsel[1], tp[0]);
      tp[1] = mfma32(asbf(fm[1]), tsel[1], tp[1]); SB();
      kc[0] = cvt8<0>(tp[0]); SB();
      y = mfma32(kc[0], vb[2][0], y); SB(); kc[1] = cvt8<8>(tp[0]); SB();
      y = mfma32(kc[1], vb[2][1], y); SB(); kc[2] = cvt8<0>(tp[1]); SB();
      y = mfma32(kc[2], vb[3][0], y); SB(); kc[3] = cvt8<8>(tp[1]); SB();
      y = mfma32(kc[3], vb[3][1], y); SB();
    }
    {
      const u32x4 yp = asu(cvt8<0>(y)), ym = asu(cvt8<8>(y));
      st16o<0>(yp, so0, rsS, kvp_own);
      st16o<512>(ym, so0, rsS, kvp_own);
      const u64 hp = hash4(yp, tag, hc), hm = hash4(ym, tag, hc + 4);
      const u32x4 pr = pair_halves(hp + hm), hz = {(unsigned)hp, (unsigned)(hp >> 32), (unsigned)hm, (unsigned)(hm >> 32)};
      u32x4 cw;
#pragma unroll
      for (int i = 0; i < 4; ++i) { const unsigned zz = (lane & 1) ? ym[i] : yp[i], cc = blk ? hz[i] : pr[i]; cw[i] = (lane & 18) == 16 ? zz : cc; }
      st16o<0>(cw, so2, rsS, kvp_own);
    }
    STAMP(16); STAMPR(54);
    issue_wo(IC(16), IC(23));
    bf16x8 wf[32];
#define RW(f) wf[f] = lds_tr(((f) < 16 ? awo : awo2) + (WO_AT(f) - ((f) < 16 ? 2 * SLOT : L_WO2)), ((f) < 16 ? awo : awo2) + (WO_AT(f) - ((f) < 16 ? 2 * SLOT : L_WO2)) + 2048)
    cnt_wait<3, 4>(cb);
#pragma unroll
    for (int hd = 0; hd < 16; ++hd) RW(hd);
    cnt_arrive<4>(cb, one);
    STAMP(17);
    unsigned spins = 0;
    waitv<7>();
    STAMP(18);
    {
      u32x4 g[4][5], zp;
      u64 zc;
#pragma unroll
      for (int i = 0; i < 4; ++i) asm volatile("" : "=v"(g[i][0]), "=v"(g[i][1]), "=v"(g[i][2]), "=v"(g[i][3]), "=v"(g[i][4]));
      asm volatile("" : "=v"(zp), "=v"(zc));
      quad_load(g[0], gv, rsS, qso[0]); quad_load(g[1], gv, rsS, qso[1]); quad_load(g[2], gv, rsS, qso[2]);
      z_load(zp, zc, zv, zcv, rsS, kvp_grp);
      quad_load(g[3], gv, rsS, qso[3]);
      const u64 tag2 = tag + tag;
      cnt_wait<4, 4>(cb);
      STAMP(19);
      const f32x4 z4 = {0.f, 0.f, 0.f, 0.f};
      f32x4 d[4][2], dz;
      unsigned bad = 0;
      auto sumq = [&](int i) {
        asm volatile("" : "+v"(g[i][0]), "+v"(g[i][1]), "+v"(g[i][2]), "+v"(g[i][3]), "+v"(g[i][4]));
        d[i][0] = mfma16(sel[0], asbf(g[i][0]), z4);
        d[i][1] = mfma16(sel[0], asbf(g[i][2]), z4);
        d[i][0] = mfma16(sel[1], asbf(g[i][1]), d[i][0]);
        d[i][1] = mfma16(sel[1], asbf(g[i][3]), d[i][1]);
        SB();
        const u64 h0 = hash4(g[i][2], hash4(g[i][0], tag2, hc), hc + 4), h1 = hash4(g[i][3], hash4(g[i][1], tag2, hc), hc + 4);
        if (UNLIKELY(anyl(h0 != mk64(g[i][4][0], g[i][4][1]) || h1 != mk64(g[i][4][2], g[i][4][3])))) { bad |= 1u << i; quad_load(g[i], gv, rsS, qso[i]); }
      };
      auto sumz = [&]() {
        asm volatile("" : "+v"(zp), "+v"(zc));
        dz = mfma16(sel[0], asbf(zp), z4);
        SB();
        if (UNLIKELY(anyl(hash4v(zp, tag, zhc) != zc))) { bad |= 16u; z_load(zp, zc, zv, zcv, rsS, kvp_grp); }
      };
      WAITV(17); sumq(0);
      WAITV(12); sumq(1);
      WAITV(7); sumq(2);
      WAITV(5); sumz();
      WAITV(0); sumq(3);
      if (UNLIKELY(bad)) {
#pragma nounroll
        do {
          WAITV(0);
          const unsigned was = bad; bad = 0;
          if (was & 1) sumq(0);
          if (was & 2) sumq(1);
          if (was & 4) sumq(2);
          if (was & 8) sumq(3);
          if (was & 16) sumz();
        } while (bad && ++spins < SPIN_CAP);
        WAITV(0);
      }
      STAMPV(62, spins);
      float s0[4], s1[4];
#pragma unroll
      for (int j = 0; j < 4; ++j) { s0[j] = add3(d[0][0][j], d[1][0][j], d[2][0][j], d[3][0][j]); s1[j] = add3(d[0][1][j], d[1][1][j], d[2][1][j], d[3][1][j]); }
      *(LAS u32x2*)(kw) = u32x2{cvt2(s0[0], s0[1]), cvt2(s0[2], s0[3])};
      *(LAS u32x2*)(kw + 640) = u32x2{cvt2(s1[0], s1[1]), cvt2(s1[2], s1[3])};
      *(LAS u32x2*)(zw) = u32x2{cvt2(dz[0], dz[1]), cvt2(dz[2], dz[3])};
    }
    STAMP(25);
    BAR();
    __builtin_amdgcn_s_setprio(1);
    STAMP(26);
    unsigned aor0 = L + L_SCR + 2048 + lane * 16, aor = L + lane * 16;
    unsigned ew0 = scr + r32 * 64 + (((2 * h) ^ ((r32 >> 1) & 3)) << 4), ew1 = ew0 ^ 16u, er = scr + (lane >> 2) * 64 + (((lane & 3) ^ ((lane >> 3) & 3)) << 4);
    asm volatile("" : "+v"(aor0), "+v"(aor), "+v"(ew0), "+v"(ew1), "+v"(er));
    RAWBAR();
    f32x16 c2[2] = {zero16(), zero16()};
    bf16x8 ob[64];
    issue_wo(IC(23), IC(32));
#pragma unroll
    for (int i = 0; i < 8; ++i) ob[i] = lds_b128(aor0 + (i & 3) * SCR + (i >> 2) * 1024);
#pragma unroll
    for (int hd = 16; hd < 23; ++hd) RW(hd);
#pragma unroll
    for (int i = 0; i < 8; ++i) c2[i >> 2] = mfma32(wf[i & 3], ob[i], c2[i >> 2]);
    SB();
    WAITV(0);
    cnt_arrive<5>(cb, one);
    cnt_wait<5, 4>(cb);
#pragma unroll
    for (int hd = 23; hd < 32; ++hd) RW(hd);
    STAMP(30);
#define RO(n) do { ob[n] = lds_b128(aor + ((n) - 8) * 1024); } while (0)
#define MO(n) do { c2[((n) >> 2) & 1] = mfma32(wf[4 * ((n) >> 3) + ((n) & 3)], ob[n], c2[((n) >> 2) & 1]); } while (0)
    RAWBAR();
    STAMP(32);
    SB();
#pragma unroll
    for (int i = 8; i < 16; ++i) RO(i);
    SB();
#pragma unroll
    for (int i = 8; i < 16; ++i) { MO(i); SB(); RO(i + 8); SB(); }
#pragma unroll
    for (int i = 16; i < 20; ++i) MO(i);
    SB();
#pragma unroll
    for (int gp = 0; gp < 2; ++gp) {
      RAWBAR();
      SB();
      const int n0 = 24 + 16 * gp;
#pragma unroll
      for (int i = 0; i < 4; ++i) { MO(n0 - 4 + i); SB(); RO(n0 + 2 * i); RO(n0 + 2 * i + 1); SB(); }
#pragma unroll
      for (int i = 0; i < 8; ++i) { MO(n0 + i); SB(); RO(n0 + 8 + i); SB(); }
      if (gp == 0) {
#pragma unroll
        for (int i = 8; i < 12; ++i) MO(n0 + i);
        SB();
      }
    }
    RAWBAR();
    STAMP(38);
    SB();
#pragma unroll
    for (int i = 0; i < 4; ++i) { MO(48 + i); SB(); RO(56 + 2 * i); RO(57 + 2 * i); SB(); }
#pragma unroll
    for (int i = 56; i < 60; ++i) MO(i);
#pragma unroll
    for (int i = 52; i < 55; ++i) MO(i);
    SB();
    auto put = [&](int t) {
      *(LAS bf16x8*)(ew0 + t * 2048) = cvt8<0>(c2[t]);
      *(LAS bf16x8*)(ew1 + t * 2048) = cvt8<8>(c2[t]);
      asm volatile("" ::: "memory");
    };
    auto sto = [&](int i, bf16x8 v) { __builtin_amdgcn_raw_buffer_store_b128(asu(v), rsY, ov, i * 32768, 16); };
    put(0);
    bf16x8 e0 = lds_b128(er), e1 = lds_b128(er + 1024);
    SB();
    MO(55);
#pragma unroll
    for (int i = 60; i < 64; ++i) MO(i);
    SB();
    sto(0, e0); sto(1, e1);
    STAMP(39);
    put(1);
    e0 = lds_b128(er + 2048); e1 = lds_b128(er + 3072);
    sto(2, e0); sto(3, e1);
  }
}

extern "C" void kernel_launch(void* const* d_in, const int* in_sizes, int n_in, void* d_out, int out_size, void* d_ws, size_t ws_size, hipStream_t stream) {
  (void)hipFuncSetAttribute((const void*)hedgehog_fused, hipFuncAttributeMaxDynamicSharedMemorySize, L_BYTES);
  hedgehog_fused<<<256, 512, L_BYTES, stream>>>((const u16*)d_in[0], (const u16*)d_in[1], (const u16*)d_in[2], (const u16*)d_in[3],
                                                (const u16*)d_in[4], (const u16*)d_in[5], (const u16*)d_in[6], (const u16*)d_in[7],
                                                (u16*)d_out, (char*)d_ws);
}
```

```cpp
#include <hip/hip_runtime.h>
#include <hip/hip_bf16.h>
#include <type_traits>
#pragma clang diagnostic ignored "-Wint-to-pointer-cast"

typedef unsigned short u16;
typedef unsigned long long u64;
typedef __bf16 bf16x8 __attribute__((ext_vector_type(8)));
typedef __bf16 bf16x4 __attribute__((ext_vector_type(4)));
typedef __bf16 bf16x2 __attribute__((ext_vector_type(2)));
typedef float f32x16 __attribute__((ext_vector_type(16)));
typedef float f32x4 __attribute__((ext_vector_type(4)));
typedef float f32x2 __attribute__((ext_vector_type(2)));
typedef unsigned u32x4 __attribute__((ext_vector_type(4)));
typedef unsigned u32x2 __attribute__((ext_vector_type(2)));
#define LAS __attribute__((address_space(3)))
#define DI __device__ __forceinline__
typedef __amdgpu_buffer_rsrc_t rsrc_t;

#define NB 3
#define XNT 0
#define NB7 7
#define C3AT 4
#define HS2 8
#define QROT 1
#define U2EARLY 1
#define RMIN 60
#define HMARG -100
#define X1 80
#define X0 110
#define PRETOUCH 1
#define HINT2 1
#define STAMPS 0

constexpr unsigned WS_EPOCH = 0;
constexpr unsigned WS_KVP   = 4096;
constexpr unsigned KVH      = 1408;
constexpr unsigned KVP_STR  = 4 * KVH;
constexpr unsigned WS_OCK   = 6u << 20;
constexpr unsigned OCK_STR  = 512;
constexpr unsigned WS_HINT  = WS_OCK + 1024 * 512;
constexpr unsigned WS_HINT1 = WS_HINT + 32768;
constexpr unsigned WS_OF    = 2u << 20;
constexpr unsigned WS_BYTES = 7u << 20;
constexpr unsigned WS_STAMP = 8u << 20;
constexpr unsigned SLOT   = 32768;
constexpr unsigned L_SCR  = 2 * SLOT;
constexpr unsigned SCR    = 6144;
constexpr unsigned L_WO2  = 2 * SLOT + 28672;
constexpr unsigned L_PHIW = L_WO2 + 65536;
constexpr unsigned L_ZERO = L_PHIW + 2048;
constexpr unsigned L_CNT  = L_ZERO + 16;
constexpr unsigned L_TAG  = L_CNT + 32;
constexpr unsigned L_TB8  = L_TAG + 8;
constexpr unsigned L_BYTES = 163840;
constexpr unsigned SPIN_CAP = 1u << 16;
constexpr unsigned OOB = 0x80000000u;
constexpr float L2E = 1.4426950408889634f;

#define WAITV(n) asm volatile("s_waitcnt vmcnt(" #n ")" ::: "memory")
#define WAITL0() asm volatile("s_waitcnt lgkmcnt(0)" ::: "memory")
#define RAWBAR() do { asm volatile("" ::: "memory"); __builtin_amdgcn_s_barrier(); asm volatile("" ::: "memory"); } while (0)
#define BAR() do { WAITL0(); __builtin_amdgcn_s_barrier(); asm volatile("" ::: "memory"); } while (0)
#define SB() __builtin_amdgcn_sched_barrier(0)
#define PIN(...) asm volatile("" : __VA_ARGS__)
#define UNLIKELY(x) __builtin_expect(!!(x), 0)
#define IC(n) std::integral_constant<int, n>{}
template <int I, int N, class F> DI void sfor(F&& f) { if constexpr (I < N) { f(std::integral_constant<int, I>{}); sfor<I + 1, N>(f); } }

template <int LI, int SI, bool NT = false> DI void dmai(unsigned lds, unsigned voff, rsrc_t rs, unsigned soff) {
  unsigned t;
  if constexpr (NT)
    asm volatile("s_add_u32 m0, %1, %5\n\ts_add_u32 %0, %4, %6\n\tbuffer_load_dwordx4 %2, %3, %0 offen nt lds"
                 : "=&s"(t) : "s"(lds), "v"(voff), "s"(rs), "s"(soff), "i"(LI), "i"(SI) : "memory", "scc");
  else
    asm volatile("s_add_u32 m0, %1, %5\n\ts_add_u32 %0, %4, %6\n\tbuffer_load_dwordx4 %2, %3, %0 offen lds"
                 : "=&s"(t) : "s"(lds), "v"(voff), "s"(rs), "s"(soff), "i"(LI), "i"(SI) : "memory", "scc");
}
template <int LI> DI void unit_load(u64& ck, unsigned lds, unsigned l16, unsigned lck, rsrc_t rs, unsigned so, unsigned sc) {
  asm volatile("s_add_u32 m0, %1, %7\n\ts_nop 0\n\t"
               "buffer_load_dwordx4 %2, %4, %5 offen sc1 lds\n\tbuffer_load_dwordx4 %2, %4, %5 offen offset:1024 sc1 lds\n\t"
               "buffer_load_dwordx4 %2, %4, %5 offen offset:2048 sc1 lds\n\tbuffer_load_dwordx4 %2, %4, %5 offen offset:3072 sc1 lds\n\t"
               "buffer_load_dwordx2 %0, %3, %4, %6 offen sc1"
               : "+v"(ck) : "s"(lds), "v"(l16), "v"(lck), "s"(rs), "s"(so), "s"(sc), "i"(LI) : "memory", "scc");
}
DI void quad_load(u32x4 (&g)[5], unsigned gv, rsrc_t rs, unsigned so) {
  asm volatile("buffer_load_dwordx4 %0, %5, %6, %7 offen sc1\n\tbuffer_load_dwordx4 %2, %5, %6, %7 offen offset:512 sc1\n\t"
               "buffer_load_dwordx4 %1, %5, %6, %7 offen offset:256 sc1\n\tbuffer_load_dwordx4 %3, %5, %6, %7 offen offset:768 sc1\n\t"
               "buffer_load_dwordx4 %4, %5, %6, %7 offen offset:1024 sc1"
               : "+v"(g[0]), "+v"(g[1]), "+v"(g[2]), "+v"(g[3]), "+v"(g[4]) : "v"(gv), "s"(rs), "s"(so) : "memory");
}
DI void z_load(u32x4& z, u64& zc, unsigned zv, unsigned zcv, rsrc_t rs, unsigned so) {
  asm volatile("buffer_load_dwordx4 %0, %2, %4, %5 offen offset:1280 sc1\n\tbuffer_load_dwordx2 %1, %3, %4, %5 offen offset:1312 sc1"
               : "+v"(z), "+v"(zc) : "v"(zv), "v"(zcv), "s"(rs), "s"(so) : "memory");
}
DI unsigned now32() { u64 t; asm volatile("s_memrealtime %0\n\ts_waitcnt lgkmcnt(0)" : "=s"(t) :: "memory"); return (unsigned)t; }
template <int N> DI void waitv() { asm volatile("s_waitcnt vmcnt(%0)" :: "i"(N) : "memory"); }
template <int OFF> DI void st16o(u32x4 v, unsigned voff, rsrc_t rs, unsigned soff) {
  asm volatile("buffer_store_dwordx4 %0, %1, %2, %3 offen offset:%4 sc1\n\ts_nop 1" :: "v"(v), "v"(voff), "s"(rs), "s"(soff), "i"(OFF) : "memory");
}
static constexpr unsigned HC[16] = {0x5E2D58Du, 0x6B8B457u, 0x7AB3C19u, 0x4C6F2A3u, 0x71D8E95u, 0x59A40CFu, 0x63F7B61u, 0x4E1936Bu,
                                    0x7F4A7C1u, 0x54D2E87u, 0x6A09E67u, 0x43C6EF3u, 0x7254FF5u, 0x510E527u, 0x69B0569u, 0x5BE0CD1u};
DI u64 hash4(u32x4 v, u64 s, const unsigned* c) {
#pragma unroll
  for (int i = 0; i < 4; ++i) s += (u64)v[i] * c[i];
  return s;
}
DI u64 hash4v(u32x4 v, u64 s, u32x4 c) {
#pragma unroll
  for (int i = 0; i < 4; ++i) s += (u64)v[i] * c[i];
  return s;
}
DI u32x4 pair_halves(u64 c) {
  auto lo = __builtin_amdgcn_permlane32_swap((unsigned)c, (unsigned)c, false, false);
  auto hi = __builtin_amdgcn_permlane32_swap((unsigned)(c >> 32), (unsigned)(c >> 32), false, false);
  return u32x4{lo[0], hi[0], lo[1], hi[1]};
}
DI u64 mk64(unsigned lo, unsigned hi) { return ((u64)hi << 32) | lo; }
DI bool anyl(bool p) { return __builtin_amdgcn_ballot_w64(p) != 0; }

DI bf16x8 lds_b128(unsigned a) { return *(const LAS bf16x8*)a; }
DI bf16x8 lds_tr(unsigned a0, unsigned a1) {
  bf16x4 lo = __builtin_amdgcn_ds_read_tr16_b64_v4bf16((LAS bf16x4*)a0);
  bf16x4 hi = __builtin_amdgcn_ds_read_tr16_b64_v4bf16((LAS bf16x4*)a1);
  return __builtin_shufflevector(lo, hi, 0, 1, 2, 3, 4, 5, 6, 7);
}
DI f32x16 mfma32(bf16x8 a, bf16x8 b, f32x16 c) { return __builtin_amdgcn_mfma_f32_32x32x16_bf16(a, b, c, 0, 0, 0); }
DI f32x4 mfma16(bf16x8 a, bf16x8 b, f32x4 c) { return __builtin_amdgcn_mfma_f32_16x16x32_bf16(a, b, c, 0, 0, 0); }
DI bf16x8 asbf(u32x4 v) { return __builtin_bit_cast(bf16x8, v); }
DI u32x4 asu(bf16x8 v) { return __builtin_bit_cast(u32x4, v); }
template <int O> DI bf16x8 cvt8(const f32x16& c) {
  bf16x8 r;
#pragma unroll
  for (int i = 0; i < 8; ++i) r[i] = (__bf16)c[O + i];
  return r;
}
DI unsigned cvt2(float a, float b) { bf16x2 v = {(__bf16)a, (__bf16)b}; return __builtin_bit_cast(unsigned, v); }
DI f32x16 zero16() { f32x16 z;
#pragma unroll
  for (int i = 0; i < 16; ++i) z[i] = 0.f;
  return z; }
DI bf16x8 onehot(bool on, int j) {
  const unsigned v = on ? (0x3F80u << (16 * (j & 1))) : 0u;
  return asbf(u32x4{(j >> 1) == 0 ? v : 0u, (j >> 1) == 1 ? v : 0u, (j >> 1) == 2 ? v : 0u, (j >> 1) == 3 ? v : 0u});
}
DI void swap4(u32x4& a, u32x4& c) {
#pragma unroll
  for (int i = 0; i < 4; ++i) { auto r = __builtin_amdgcn_permlane32_swap(a[i], c[i], false, false); a[i] = r[0]; c[i] = r[1]; }
}

DI float max16(const f32x16& p) {
  float m = __builtin_elementwise_maximum(p[0], p[1]);
#pragma unroll
  for (int i = 2; i < 16; i += 2) m = __builtin_elementwise_maximum(__builtin_elementwise_maximum(m, p[i]), p[i + 1]);
  return m;
}
DI float minus_exact(const f32x16& p, float (&g)[16]) {
  float mn = p[0];
#pragma unroll
  for (int i = 1; i < 16; ++i) mn = __builtin_fminf(mn, p[i]);
  float s = 0.f;
#pragma unroll
  for (int i = 0; i < 16; ++i) { g[i] = __builtin_amdgcn_exp2f((mn - p[i]) * L2E); s += g[i]; }
  return s;
}
constexpr float BIG = 0x1p120f;
DI float sum16pk(const float (&e)[16]) {
  f32x2 t[8];
#pragma unroll
  for (int i = 0; i < 8; ++i) t[i] = f32x2{e[2 * i], e[2 * i + 1]};
  const f32x2 s = ((t[0] + t[1]) + (t[2] + t[3])) + ((t[4] + t[5]) + (t[6] + t[7]));
  return s.x + s.y;
}
DI void args16pk(const f32x16& p, float mx, float (&x)[16]) {
  const float a = -mx * L2E;
#pragma unroll
  for (int i = 0; i < 8; ++i) { const f32x2 v = f32x2{p[2 * i], p[2 * i + 1]} * f32x2{L2E, L2E} + f32x2{a, a}; x[2 * i] = v.x; x[2 * i + 1] = v.y; }
}
DI void scale16pk(float (&e)[16], float r) {
#pragma unroll
  for (int i = 0; i < 8; ++i) { const f32x2 v = f32x2{e[2 * i], e[2 * i + 1]} * f32x2{r, r}; e[2 * i] = v.x; e[2 * i + 1] = v.y; }
}
DI void pack16(const float (&e)[16], u32x4& a, u32x4& c) {
#pragma unroll
  for (int i = 0; i < 4; ++i) { a[i] = cvt2(e[2 * i], e[2 * i + 1]); c[i] = cvt2(e[8 + 2 * i], e[9 + 2 * i]); }
}
template <int I0, int I1> DI void exp_r(const float (&x)[16], float (&e)[16]) {
#pragma unroll
  for (int i = I0; i < I1; ++i) e[i] = __builtin_amdgcn_exp2f(x[i]);
}
template <int I0, int I1> DI void rcp_r(const float (&e)[16], float (&g)[16]) {
#pragma unroll
  for (int i = I0; i < I1; ++i) g[i] = __builtin_amdgcn_rcpf(e[i]);
}
DI void fma4(float (&x)[16], int o, const f32x16& p, float c, float a) {
  asm("v_fma_f32 %0, %4, %8, %9\n\tv_fma_f32 %1, %5, %8, %9\n\tv_fma_f32 %2, %6, %8, %9\n\tv_fma_f32 %3, %7, %8, %9"
      : "=&v"(x[o]), "=&v"(x[o + 1]), "=&v"(x[o + 2]), "=&v"(x[o + 3]) : "v"(p[o]), "v"(p[o + 1]), "v"(p[o + 2]), "v"(p[o + 3]), "s"(c), "v"(a));
}
DI float add4(float s, const float (&e)[16], int o) {
  float r;
  asm("v_add_f32_e32 %0, %1, %2\n\tv_add_f32_e32 %0, %0, %3\n\tv_add_f32_e32 %0, %0, %4\n\tv_add_f32_e32 %0, %0, %5"
      : "=&v"(r) : "v"(s), "v"(e[o]), "v"(e[o + 1]), "v"(e[o + 2]), "v"(e[o + 3]));
  return r;
}
DI float add3(float a, float b, float c, float d) {
  float r;
  asm("v_add_f32_e32 %0, %1, %2\n\tv_add_f32_e32 %0, %0, %3\n\tv_add_f32_e32 %0, %0, %4" : "=&v"(r) : "v"(a), "v"(b), "v"(c), "v"(d));
  return r;
}
DI float addpp(float a, float b, float c, float d) {
  float r, t;
  asm("v_add_f32_e32 %0, %2, %3\n\tv_add_f32_e32 %1, %4, %5\n\tv_add_f32_e32 %0, %0, %1" : "=&v"(r), "=&v"(t) : "v"(a), "v"(b), "v"(c), "v"(d));
  return r;
}
DI u32x2 mulcvt4(float a, float b, float c, float d, float r) {
  unsigned x, y; float t0, t1;
  asm("v_mul_f32_e32 %0, %4, %8\n\tv_mul_f32_e32 %2, %5, %8\n\tv_mul_f32_e32 %1, %6, %8\n\tv_mul_f32_e32 %3, %7, %8\n\t"
      "v_cvt_pk_bf16_f32 %0, %0, %2\n\tv_cvt_pk_bf16_f32 %1, %1, %3"
      : "=&v"(x), "=&v"(y), "=&v"(t0), "=&v"(t1) : "v"(a), "v"(b), "v"(c), "v"(d), "v"(r));
  return u32x2{x, y};
}

DI u32x4 mulcvt8(const f32x16& n, float r) {
  u32x4 o; float t0, t1, t2, t3;
  asm("v_mul_f32_e32 %0, %8, %16\n\tv_mul_f32_e32 %4, %9, %16\n\tv_mul_f32_e32 %1, %10, %16\n\tv_mul_f32_e32 %5, %11, %16\n\t"
      "v_mul_f32_e32 %2, %12, %16\n\tv_mul_f32_e32 %6, %13, %16\n\tv_mul_f32_e32 %3, %14, %16\n\tv_mul_f32_e32 %7, %15, %16\n\t"
      "v_cvt_pk_bf16_f32 %0, %0, %4\n\tv_cvt_pk_bf16_f32 %1, %1, %5\n\tv_cvt_pk_bf16_f32 %2, %2, %6\n\tv_cvt_pk_bf16_f32 %3, %3, %7"
      : "=&v"(o[0]), "=&v"(o[1]), "=&v"(o[2]), "=&v"(o[3]), "=&v"(t0), "=&v"(t1), "=&v"(t2), "=&v"(t3)
      : "v"(n[0]), "v"(n[1]), "v"(n[2]), "v"(n[3]), "v"(n[4]), "v"(n[5]), "v"(n[6]), "v"(n[7]), "v"(r));
  return o;
}

#define STAMP(i) do {} while (0)
#define STAMPR(i) do {} while (0)
#define STAMPV(i, x) do {} while (0)

template <int C> DI void cnt_arrive(unsigned base, unsigned one) {
  asm volatile("s_mov_b64 exec, 1\n\tds_add_u32 %0, %1 offset:%2\n\ts_mov_b64 exec, -1" :: "v"(base), "v"(one), "i"(4 * C) : "memory");
}
template <int C, int N> DI void cnt_wait(unsigned base) {
  unsigned v, t;
  asm volatile("s_branch 2f\n1:\n\ts_sleep 1\n2:\n\tds_read_b32 %0, %2 offset:%3\n\ts_waitcnt lgkmcnt(0)\n\tv_readfirstlane_b32 %1, %0\n\ts_nop 0\n\ts_cmp_lt_u32 %1, %4\n\ts_cbranch_scc1 1b"
               : "=&v"(v), "=&s"(t) : "v"(base), "i"(4 * C), "i"(N) : "memory", "scc");
}

__global__ __launch_bounds__(512, 2) void hedgehog_fused(
    const u16* __restrict__ xA, const u16* __restrict__ wA, const u16* __restrict__ pA, const u16* __restrict__ oA,
    const u16* __restrict__ xB, const u16* __restrict__ wB, const u16* __restrict__ pB, const u16* __restrict__ oB,
    u16* __restrict__ out, char* __restrict__ ws) {
  extern __shared__ __attribute__((aligned(16))) char smem[];
  STAMPR(60); STAMP(0);
  const unsigned L = (unsigned)(size_t)(LAS char*)smem;
  const int b = blockIdx.x, xcd = b & 7, jj = b >> 3;
#define MAP 1
#define M0 0
  const int br = xcd >> 2;
  const int rt = MAP ? ((xcd >> 1) & 1) * 8 + (jj >> 2) : (xcd & 3) * 4 + (jj >> 3), hg = MAP ? (xcd & 1) * 4 + (jj & 3) : jj & 7;
  const int rot = MAP ? (2 * hg + rt + (xcd & 1)) & 7 : (hg + 2 * (rt & 3)) & 7;
  const int dir = MAP ? 1 - 2 * (rt & 1) : 1;
  auto chunk_of = [&](int v) -> unsigned { return (unsigned)((v + rot) & 7); };
  const int tid = threadIdx.x, lane = tid & 63;
  const int w = __builtin_amdgcn_readfirstlane(tid >> 6);
  const int u = w & 3;
  const int r32 = lane & 31, h = lane >> 5, blk = (lane >> 4) & 1, q4 = (lane & 15) >> 2, p4 = lane & 3;
  const unsigned fx = (unsigned)(h ^ ((r32 >> 1) & 7));
#define MAKE_WO() \
  rsrc_t rsO = __builtin_amdgcn_make_buffer_rsrc((void*)(br ? oB : oA), 0, 512 * 1024, 0x00020000); \
  rsrc_t rsS = __builtin_amdgcn_make_buffer_rsrc((void*)ws, 0, WS_BYTES, 0x00020000); \
  PIN("+s"(rsO), "+s"(rsS)); \
  unsigned vwo = (unsigned)((lane >> 4) * 1024 + (hg & 3) * 256 + (((lane & 15) ^ ((lane >> 4) << 2)) << 4)); \
  unsigned cb = L + L_CNT, one = 1; asm volatile("" : "+v"(cb), "+v"(one), "+v"(vwo)); \
  unsigned lwo = L + u * 1024; PIN("+s"(lwo)); \
  auto issue_wo = [&](auto F0, auto F1) {     \
    sfor<decltype(F0)::value, decltype(F1)::value>([&](auto F) { constexpr int f = decltype(F)::value; \
      dmai<WO_AT(f), (f & 3) * 16384>(lwo, vwo, rsO, (unsigned)(((hg + dir * (f >> 2)) & 7) * 65536 + u * 4096)); }); \
  };
#define WO_AT(f) ((f) < 16 ? 2 * SLOT + (f) * 4096 : ((f) < 23 ? L_WO2 + ((f) - 7) * 4096 : L_WO2 + ((f) - 23) * 4096))

  if (__builtin_expect(w >= 4, 1)) {
    const rsrc_t rsX = __builtin_amdgcn_make_buffer_rsrc((void*)((br ? xB : xA) + (size_t)rt * 128 * 512), 0, 128 * 1024, 0x00020000);
    const rsrc_t rsW = __builtin_amdgcn_make_buffer_rsrc((void*)(br ? wB : wA), 0, 512 * 3072, 0x00020000);
    const unsigned vxe = (unsigned)((lane >> 3) * 1024 + (((lane & 7) ^ (lane >> 4)) << 4));
    const unsigned vxo = vxe ^ 64u;
    const unsigned vwk = (unsigned)((lane >> 3) * 3072 + hg * 128 + (((lane & 7) ^ (((lane >> 4) & 1) << 2)) << 4));
    const unsigned vwv = vwk ^ 32u;
    const unsigned lx = L + u * 4096, lw = L + u * 2048;
    auto issue = [&](auto V, auto I0, auto I1) {
      constexpr int v = decltype(V)::value;
      if constexpr (v < 8) {
        const unsigned c = chunk_of(v);
        const unsigned sx = c * 128 + u * 32768, sw = c * (64 * 3072) + u * (16 * 3072);
        sfor<decltype(I0)::value, decltype(I1)::value>([&](auto I) {
          constexpr int i = decltype(I)::value;
          if constexpr (i < 4) dmai<(v & 3) * SLOT + i * 1024, i * 8192, (v < XNT)>(lx, (i & 1) ? vxo : vxe, rsX, sx);
          else if constexpr (i < 6) dmai<(v & 3) * SLOT + 16384 + (i - 4) * 1024, (i - 4) * (8 * 3072) + 1024>(lw, vwk, rsW, sw);
          else dmai<(v & 3) * SLOT + 24576 + (i - 6) * 1024, (i - 6) * (8 * 3072) + 2048>(lw, vwv, rsW, sw);
        });
      } else {
        sfor<decltype(I0)::value, decltype(I1)::value>([&](auto I) {
          constexpr int i = decltype(I)::value;
          const unsigned c = chunk_of(4 * (v - 8) + (i >> 1));
          dmai<(v - 8) * SLOT + (i >> 1) * 8192 + (i & 1) * 1024, (i & 1) * (8 * 3072)>(lw, vwk, rsW, c * (64 * 3072) + u * (16 * 3072));
        });
      }
    };
    asm volatile("s_nop 4" ::: "memory");
    STAMP(50);
    issue(IC(0), IC(0), IC(8)); issue(IC(1), IC(0), IC(3));
    MAKE_WO();
    u64 tag;
    bf16x8 xf[32], ap[4];
    unsigned axk[2][4];
#pragma unroll
    for (int ks = 0; ks < 4; ++ks) { axk[0][ks] = L + (32 * u + r32) * 128 + ((fx ^ (2 * ks)) << 4); axk[1][ks] = axk[0][ks] + 2 * SLOT; asm volatile("" : "+v"(axk[1][ks])); }
    unsigned aw0 = L + (8 * h + q4) * 128 + (((2 * blk + (p4 & 1)) ^ ((q4 >> 1) << 2)) << 4) + 8 * (p4 >> 1), aw1 = aw0 ^ 64u;
    unsigned apb = L + L_PHIW + (8 * h + q4) * 32 + (2 * blk + (p4 >> 1)) * 8;
    asm volatile("" : "+v"(aw0), "+v"(aw1), "+v"(apb));
    unsigned hc[16];
#pragma unroll
    for (int i = 0; i < 16; ++i) { hc[i] = HC[i]; asm volatile("" : "+s"(hc[i])); }
    unsigned kb = L + L_SCR + h * 320 + (r32 < 16 ? r32 : 16 + (r32 & 3)) * 16;
    unsigned aown = L + L_SCR + 2048 + (u & 1) * 1024 + lane * 16;
    unsigned l16 = lane * 16, l16h = h ? OOB : l16, lck = r32 * 16 + h * 8, avf = lx + lane * 16;
    asm volatile("" : "+v"(kb), "+v"(aown), "+v"(l16), "+v"(l16h), "+v"(lck), "+v"(avf));
    unsigned gso[4], gsc[4], hv;
    u64 pre = 0;
    {
      const unsigned tt2 = (unsigned)(br * 64 + 4 * rt + 2 * (hg >> 2) + (u & 1));
#pragma unroll
      for (int g = 0; g < 4; ++g) {
        const unsigned pr = (unsigned)(hg + dir * (2 * g + 1 + (u >> 1))) & 7;
        gso[g] = WS_OF + (tt2 * 32 + 4 * pr) * 1024; gsc[g] = WS_OCK + (tt2 * 8 + pr) * OCK_STR;
        asm volatile("" : "+s"(gso[g]), "+s"(gsc[g]));
      }
      hv = WS_HINT + ((unsigned)(br * 16 + rt) * 8 + ((unsigned)(hg + dir * (2 * (lane & 3) + 1 + (u >> 1))) & 7)) * 128;
      PIN("+v"(hv));
    }
    unsigned of_own = WS_OF + (unsigned)(((br * 64 + 4 * rt + u) * 32 + 4 * hg) * 1024), ock = WS_OCK + (unsigned)((br * 64 + 4 * rt + u) * 8 + hg) * OCK_STR;
    unsigned h1v = lane < 8 ? lane * 16 : OOB, h1o = WS_HINT1 + (unsigned)((br * 8 + hg) * 16 + rt) * 128;
    asm volatile("" : "+s"(of_own), "+s"(ock), "+v"(h1v), "+s"(h1o));
    SB();
    issue(IC(1), IC(3), IC(8));
    sfor<0, 8>([&](auto S) {
      constexpr int s = decltype(S)::value;
      if constexpr (s < 7) { issue(IC(s + 2), IC(0), IC(NB)); waitv<8 + NB>(); }
      else waitv<NB7>();
      BAR();
      if (s == 0) { STAMP(2); STAMPR(52); } if (s == 7) { STAMP(9); STAMPR(53); }
      if constexpr (s < 6) issue(IC(s + 2), IC(NB), IC(8));
      if constexpr (s == 6) issue(IC(8), IC(NB), IC(NB7));
      if constexpr (s == 7) { issue(IC(8), IC(NB7), IC(8)); issue(IC(9), IC(0), IC(5)); }
      if (s == 1) tag = *(const LAS u64*)(L + L_TAG);
      if constexpr (s >= 2 && s < 6) {
        const bool on = (p4 & 1) == (s & 1);
        ap[s - 2] = lds_tr(on ? apb + (s - 2) * 512 : L + L_ZERO, on ? apb + (s - 2) * 512 + 128 : L + L_ZERO);
      }
#pragma unroll
      for (int ks = 0; ks < 4; ++ks) xf[4 * s + ks] = lds_b128(axk[(s >> 1) & 1][ks] + (s & 1) * SLOT);
      SB();
    });
    cnt_arrive<0>(cb, one);
    issue(IC(9), IC(5), IC(8));
    issue_wo(IC(0), IC(8));
    STAMP(10);
    waitv<8>();
    cnt_arrive<1>(cb, one);
    cnt_wait<0, 8>(cb);
    issue_wo(IC(8), IC(16));
    { u64 f; asm volatile("buffer_load_dwordx2 %0, %1, %2, 0 offen sc1" : "=v"(f) : "v"(hv), "s"(rsS) : "memory"); pre = f; }
    STAMP(11);
    cnt_wait<1, 4>(cb);
    bf16x8 wq[4];
#define RQ(n) wq[(n) & 3] = lds_tr(((n) < 32 ? aw0 : aw1) + ((n) & 31) * 2048, ((n) < 32 ? aw0 : aw1) + ((n) & 31) * 2048 + 512)
    RQ(0); RQ(1); RQ(2);
    cnt_wait<2, 4>(cb);
    if (u == 3) { const unsigned t = now32(); st16o<0>(u32x4{(unsigned)tag, (unsigned)(tag >> 32), t, t}, h1v, rsS, h1o); }
    __builtin_amdgcn_s_setprio(1);
    STAMP(12);
    f32x16 q0 = zero16(), q1 = zero16();
    SB();
    sfor<0, 32>([&](auto N) {
      constexpr int n = decltype(N)::value;
      RQ(n + 3); q0 = mfma32(wq[n & 3], xf[n], q0); SB();
      if constexpr (n == C3AT) { if (u == 3) waitv<1>(); else waitv<0>(); cnt_arrive<3>(cb, one); SB(); }
    });
    u32x4 fq[4][2];
    {
      f32x16 p;
      float x[16], e[16], g[16], ca, se, sg, re, rg;
      bf16x8 c0, c1;
      sfor<32, 64>([&](auto N) {
        constexpr int n = decltype(N)::value, i = n - 32;
        if constexpr (n + 3 < 64) RQ(n + 3);
        q1 = mfma32(wq[n & 3], xf[i], q1);
        SB();
        if constexpr (i == 2) c0 = cvt8<0>(q0);
        if constexpr (i == 3) { c1 = cvt8<8>(q0); p = mfma32(ap[0], c0, zero16()); }
        if constexpr (i == 4) p = mfma32(ap[1], c1, p);
        if constexpr (i == 8) ca = -max16(p) * L2E;
        if constexpr (i == 9) { fma4(x, 0, p, L2E, ca); fma4(x, 4, p, L2E, ca); }
        if constexpr (i == 10) { exp_r<0, 3>(x, e); }
        if constexpr (i == 11) { fma4(x, 8, p, L2E, ca); exp_r<3, 5>(x, e); }
        if constexpr (i == 12) { fma4(x, 12, p, L2E, ca); exp_r<5, 7>(x, e); }
        if constexpr (i >= 13 && i < 16) exp_r<7 + 3 * (i - 13), 10 + 3 * (i - 13)>(x, e);
        if constexpr (i == 16) { rcp_r<0, 2>(e, g); se = add3(e[0], e[1], e[2], e[3]); }
        if constexpr (i >= 17 && i < 20) { rcp_r<2 * (i - 16), 2 * (i - 15)>(e, g); se = add4(se, e, 4 * (i - 16)); }
        if constexpr (i == 20) { re = __builtin_amdgcn_rcpf(se); PIN("+v"(re)); rcp_r<8, 10>(e, g); sg = add3(g[0], g[1], g[2], g[3]); }
        if constexpr (i == 21) { rcp_r<10, 12>(e, g); sg = add4(sg, g, 4); }
        if constexpr (i == 22) { rcp_r<12, 16>(e, g); }
        if constexpr (i == 23) { sg = add4(sg, g, 8); sg = add4(sg, g, 12); }
        if constexpr (i == 24) { if (UNLIKELY(anyl(!(sg < BIG)))) sg = minus_exact(p, g); rg = __builtin_amdgcn_rcpf(sg); }
        if constexpr (i >= 24 && i < 28) { constexpr int k = i - 24; const u32x2 t = mulcvt4(e[4 * k], e[4 * k + 1], e[4 * k + 2], e[4 * k + 3], re); fq[k >> 1][0][2 * (k & 1)] = t.x; fq[k >> 1][0][2 * (k & 1) + 1] = t.y; }
        if constexpr (i >= 28) { constexpr int k = i - 28; const u32x2 t = mulcvt4(g[4 * k], g[4 * k + 1], g[4 * k + 2], g[4 * k + 3], rg); fq[k >> 1][1][2 * (k & 1)] = t.x; fq[k >> 1][1][2 * (k & 1) + 1] = t.y; }
        SB();
      });
      STAMP(13);
      asm volatile("s_nop 1" : "+v"(fq[0][0]), "+v"(fq[1][0]), "+v"(fq[0][1]), "+v"(fq[1][1]));
      swap4(fq[0][0], fq[1][0]);
      SB();
      c0 = cvt8<0>(q1); c1 = cvt8<8>(q1);
      p = mfma32(ap[2], c0, zero16());
      p = mfma32(ap[3], c1, p);
      SB();
      swap4(fq[0][1], fq[1][1]);
      SB();
      args16pk(p, max16(p), x);
      exp_r<0, 16>(x, e); rcp_r<0, 16>(e, g);
      se = sum16pk(e); sg = sum16pk(g); PIN("+v"(se));
      if (UNLIKELY(anyl(!(sg < BIG)))) sg = minus_exact(p, g);
      scale16pk(e, __builtin_amdgcn_rcpf(se)); scale16pk(g, __builtin_amdgcn_rcpf(sg));
      pack16(e, fq[2][0], fq[3][0]); pack16(g, fq[2][1], fq[3][1]);
      swap4(fq[2][0], fq[3][0]); swap4(fq[2][1], fq[3][1]);
    }
#pragma unroll
    for (int a = 0; a < 4; ++a) asm volatile("" : "+v"(fq[a][0]), "+v"(fq[a][1]));
    __builtin_amdgcn_s_setprio(3);
    asm volatile("" :: "v"(pre));
    STAMP(14);
    RAWBAR();
    STAMP(26); STAMPR(55);
    u32x4 hf;
    {
      bf16x8 ka[4][2];
#pragma unroll
      for (int a = 0; a < 4; ++a) { ka[a][0] = lds_b128(kb + a * SCR); ka[a][1] = lds_b128(kb + a * SCR + 640); }
      SB();
      f32x16 n[4];
      u32x4 ou[4];
      u64 hs = tag;
      auto mm = [&](int a) { n[a] = mfma32(ka[a][0], asbf(fq[a][0]), zero16()); n[a] = mfma32(ka[a][1], asbf(fq[a][1]), n[a]); SB(); };
      float rd[4];
      auto den = [&](int a) { rd[a] = __builtin_amdgcn_rcpf((((n[a][8] + n[a][9]) + n[a][10]) + n[a][11]) + 1e-6f); SB(); };
      auto fin = [&](auto A) { constexpr int a = decltype(A)::value; ou[a] = mulcvt8(n[a], rd[a]); st16o<a * 1024>(ou[a], l16, rsS, of_own); SB(); };
      mm(0); mm(1); mm(2);
      den(0);
      mm(3);
      fin(IC(0)); den(1); hs = hash4(ou[0], hs, hc); PIN("+v"(hs)); SB();
      fin(IC(1)); den(2); hs = hash4(ou[1], hs, hc + 4); PIN("+v"(hs)); SB();
      fin(IC(2)); den(3); hs = hash4(ou[2], hs, hc + 8); PIN("+v"(hs)); SB();
      fin(IC(3)); hs = hash4(ou[3], hs, hc + 12);
      asm volatile("" :: "v"(n[0]), "v"(n[1]), "v"(n[2]), "v"(n[3]));
      st16o<0>(pair_halves(hs), l16h, rsS, ock);
      asm volatile("buffer_load_dwordx4 %0, %1, %2, 0 offen sc1" : "=v"(hf) : "v"(hv), "s"(rsS) : "memory");
      if ((u >> 1) == (hg >> 2)) {
#pragma unroll
        for (int a = 0; a < 4; ++a) *(LAS u32x4*)(aown + a * SCR) = ou[a];
      }
    }
    STAMP(27);
    BAR();
    unsigned spins2 = 0;
    const unsigned tb8 = *(const LAS unsigned*)(L + L_TB8);
    STAMP(28);
    WAITV(0);
    PIN("+v"(hf));
    if (UNLIKELY(anyl(mk64(hf[0], hf[1]) != tag || (int)(hf[2] - tb8) > HMARG))) {
#pragma nounroll
      while (anyl(mk64(hf[0], hf[1]) != tag) && ++spins2 < SPIN_CAP)
        asm volatile("buffer_load_dwordx4 %0, %1, %2, 0 offen sc1\n\ts_waitcnt vmcnt(0)" : "+v"(hf) : "v"(hv), "s"(rsS) : "memory");
#pragma nounroll
      for (int k = 0; k < 64 && anyl((int)(hf[2] + X0 - now32()) > 0); ++k) {}
    }
    STAMP(29);
    u64 ck[4];
    asm volatile("" : "=v"(ck[0]), "=v"(ck[1]), "=v"(ck[2]), "=v"(ck[3]));
    auto verify = [&](auto G) {
      constexpr int g = decltype(G)::value;
      auto bad = [&]() {
        asm volatile("" : "+v"(ck[g]) :: "memory");
        const u32x4 v0 = *(const LAS u32x4*)(avf + g * 16384), v1 = *(const LAS u32x4*)(avf + g * 16384 + 1024);
        const u32x4 v2 = *(const LAS u32x4*)(avf + g * 16384 + 2048), v3 = *(const LAS u32x4*)(avf + g * 16384 + 3072);
        return anyl(hash4(v3, hash4(v2, hash4(v1, hash4(v0, tag, hc), hc + 4), hc + 8), hc + 12) != ck[g]);
      };
      if (UNLIKELY(bad())) {
#pragma nounroll
        do { unit_load<g * 16384>(ck[g], lx, l16, lck, rsS, gso[g], gsc[g]); WAITV(0); } while (++spins2 < SPIN_CAP && bad());
      }
    };
    unit_load<0>(ck[0], lx, l16, lck, rsS, gso[0], gsc[0]);
    unit_load<16384>(ck[1], lx, l16, lck, rsS, gso[1], gsc[1]);
    unit_load<32768>(ck[2], lx, l16, lck, rsS, gso[2], gsc[2]);
    STAMP(30);
    waitv<10>();
    STAMP(31);
    verify(IC(0));
    RAWBAR();
    STAMP(32); STAMPR(56);
    if (u < 2) unit_load<49152>(ck[3], lx, l16, lck, rsS, gso[3], gsc[3]);
    if (u < 2) waitv<10>(); else waitv<5>();
    verify(IC(1));
    STAMP(33);
    RAWBAR();
    if (u < 2) waitv<5>(); else waitv<0>();
    verify(IC(2));
    STAMP(34);
    RAWBAR();
    if (u < 2) { waitv<0>(); verify(IC(3)); }
    STAMP(35);
    RAWBAR();
    STAMP(38);
    STAMPV(63, spins2);
    if (rt == 0 && hg == 0 && u == 0)
      asm volatile("buffer_store_dwordx2 %0, %1, %2, %3 offen sc1" :: "v"(tag), "v"(lane == 0 ? 0u : OOB), "s"(rsS), "s"(WS_EPOCH + br * 128) : "memory");
  } else {
    {
      const rsrc_t rsP = __builtin_amdgcn_make_buffer_rsrc((void*)(br ? pB : pA), 0, 32 * 512, 0x00020000);
      asm volatile("s_nop 4" ::: "memory");
      if (u < 2) dmai<L_PHIW, 0>(L + u * 1024, (unsigned)(lane * 16), rsP, (unsigned)(hg * 2048 + u * 1024));
      if (u == 3) {
        u64 e;
        asm volatile("s_load_dwordx2 %0, %1, 0x0\n\ts_waitcnt lgkmcnt(0)" : "=&s"(e) : "s"(ws + WS_EPOCH + br * 128) : "memory");
        if (lane < 3) *(LAS u32x4*)(L + L_ZERO + lane * 16) = u32x4{0, 0, 0, 0};
        if (lane == 3) *(LAS u64*)(L + L_TAG) = e + 1;
      }
    }
    MAKE_WO();
    f32x16 acc[4] = {zero16(), zero16(), zero16(), zero16()};
    unsigned axk[2][4];
#pragma unroll
    for (int ks = 0; ks < 4; ++ks) { axk[0][ks] = L + r32 * 128 + ((fx ^ (2 * ks)) << 4); axk[1][ks] = axk[0][ks] + 2 * SLOT; asm volatile("" : "+v"(axk[0][ks]), "+v"(axk[1][ks])); }
    unsigned awb[2];
    awb[0] = L + 16384 + blk * 8192 + (8 * h + q4) * 128 + (((2 * u + (p4 & 1)) ^ ((q4 >> 1) << 2) ^ (blk << 1)) << 4) + 8 * (p4 >> 1);
    awb[1] = awb[0] + 2 * SLOT; asm volatile("" : "+v"(awb[0]), "+v"(awb[1]));
    const unsigned scr = L + L_SCR + u * SCR;
    const int jt = (lane & 3) + 4 * ((lane >> 3) & 1);
    unsigned so0, so2, apa[2][2];
    {
      const unsigned dcol = 8 * ((lane >> 2) & 1) + jt;
      so0 = blk == 0 ? h * 256 + dcol * 16 : OOB;
      so2 = blk == 0 ? (h ? OOB : 1024 + dcol * 16) : ((lane & 15) < 4 ? 1280u + h * 64 + p4 * 16 : OOB);
      asm volatile("" : "+v"(so0), "+v"(so2));
#pragma unroll
      for (int s = 0; s < 2; ++s) {
        const unsigned base = L + L_PHIW + u * 512 + (8 * h + q4) * 32 + (2 * blk + (p4 >> 1)) * 8;
        const bool on = (p4 & 1) == s;
        apa[s][0] = on ? base : L + L_ZERO; apa[s][1] = on ? base + 128 : L + L_ZERO;
        asm volatile("" : "+v"(apa[s][0]), "+v"(apa[s][1]));
      }
    }
    bf16x8 tsel[2] = {onehot(((lane >> 2) & 1) == h && blk == 0, jt), onehot(((lane >> 2) & 1) == h && blk == 1, jt)};
    bf16x8 sel[2] = {onehot(((lane >> 3) & 1) == 0, lane & 7), onehot(((lane >> 3) & 1) == 1, lane & 7)};
    f32x16 cone;
#pragma unroll
    for (int i = 0; i < 16; ++i) cone[i] = blk ? 1.f : 0.f;
    asm volatile("" : "+v"(tsel[0]), "+v"(tsel[1]), "+v"(sel[0]), "+v"(sel[1]), "+v"(cone));
    unsigned awo = L + 2 * SLOT + (4 * h + q4) * 256 + (((4 * u + 2 * (p4 & 1) + blk) ^ (q4 << 2)) << 4) + 8 * (p4 >> 1);
    unsigned awo2 = awo + (L_WO2 - 2 * SLOT); PIN("+v"(awo2));
    unsigned gv = (unsigned)((lane >> 4) * KVP_STR + (lane & 15) * 16);
    unsigned zv = (unsigned)((lane >> 2) * KVP_STR + ((lane >> 1) & 1) * 64 + (lane & 1) * 16), zcv = zv - (lane & 1) * 8;
    unsigned kw = scr + h * 320 + (lane & 15) * 16 + blk * 8;
    unsigned zw = h ? scr + 1280 + lane * 8 : scr + (2 * (lane & 1) + ((lane >> 1) & 1)) * 320 + 256 + ((lane >> 2) & 3) * 16 + blk * 8;
    unsigned ov = (unsigned)((lane >> 2) * 2048 + (lane & 3) * 16);
    rsrc_t rsY = __builtin_amdgcn_make_buffer_rsrc((void*)(out + (size_t)(rt * 128 + (hg >> 2) * 64) * 1024 + br * 512 + (hg & 3) * 128 + u * 32), 0, 63 * 2048 + 64, 0x00020000);
    asm volatile("" : "+v"(awo), "+v"(gv), "+v"(zv), "+v"(zcv), "+v"(kw), "+v"(zw), "+v"(ov), "+s"(rsY));
    unsigned kvp_grp = WS_KVP + (unsigned)((br * 8 + hg) * 16) * KVP_STR + u * KVH, kvp_own = kvp_grp + rt * KVP_STR;
    unsigned hc[8], h1v = WS_HINT1 + (unsigned)((br * 8 + hg) * 16 + (lane & 15)) * 128;
    PIN("+v"(h1v));
#pragma unroll
    for (int i = 0; i < 8; ++i) { hc[i] = HC[i]; asm volatile("" : "+s"(hc[i])); }
    unsigned hsv = lane < 8 ? lane * 16 : OOB, hso = WS_HINT + (unsigned)((br * 16 + rt) * 8 + hg) * 128;
    PIN("+v"(hsv), "+s"(hso));
    u32x4 zhc;
#pragma unroll
    for (int i = 0; i < 4; ++i) zhc[i] = HC[i] + (unsigned)(lane & 1) * (HC[4 + i] - HC[i]);
    asm volatile("" : "+s"(kvp_grp), "+s"(kvp_own), "+v"(zhc));
    u64 tag;
    bf16x8 fa[3], fb[3][4], ap[2];
    WAITV(0);
    STAMP(1);
#pragma unroll
    for (int s = 0; s < 8; ++s) {
      BAR();
      if (s == 0) STAMP(2); if (s == 7) STAMP(9);
      const unsigned sl = (s & 1) * SLOT;
#define RD_A(ks) fa[(4 * s + (ks)) % 3] = lds_tr(awb[(s >> 1) & 1] + sl + (ks) * 2048, awb[(s >> 1) & 1] + sl + (ks) * 2048 + 512)
#define RD_B(ks, t) fb[(4 * s + (ks)) % 3][t] = lds_b128(axk[(s >> 1) & 1][ks] + sl + (t) * 4096)
#define MM(n, t) acc[t] = mfma32(fa[(n) % 3], fb[(n) % 3][t], acc[t])
      SB();
      if (s > 0) {
        MM(4 * s - 1, 0); SB(); RD_A(0); RD_B(0, 0); SB();
        MM(4 * s - 1, 1); SB(); RD_B(0, 1); RD_B(0, 2); RD_B(0, 3); SB();
        MM(4 * s - 1, 2); SB(); RD_A(1); RD_B(1, 0); SB();
        MM(4 * s - 1, 3); SB(); RD_B(1, 1); RD_B(1, 2); RD_B(1, 3); SB();
      } else {
        RD_A(0); RD_B(0, 0); RD_B(0, 1); RD_B(0, 2); RD_B(0, 3); RD_A(1); RD_B(1, 0); RD_B(1, 1); RD_B(1, 2); RD_B(1, 3); SB();
      }
      MM(4 * s, 0); SB(); RD_A(2); SB();
      MM(4 * s, 1); SB(); RD_B(2, 0); RD_B(2, 1); SB();
      MM(4 * s, 2); SB(); RD_B(2, 2); RD_B(2, 3); SB();
      MM(4 * s, 3); SB();
      MM(4 * s + 1, 0); SB(); RD_A(3); SB();
      MM(4 * s + 1, 1); SB(); RD_B(3, 0); RD_B(3, 1); SB();
      MM(4 * s + 1, 2); SB(); RD_B(3, 2); RD_B(3, 3); SB();
      MM(4 * s + 1, 3); SB();
      if (s == 7) { cnt_arrive<0>(cb, one); SB(); }
      if (s == 1) { ap[0] = lds_tr(apa[0][0], apa[0][1]); ap[1] = lds_tr(apa[1][0], apa[1][1]); tag = *(const LAS u64*)(L + L_TAG); SB(); }
      if (s < 7) { MM(4 * s + 2, 0); MM(4 * s + 2, 1); MM(4 * s + 2, 2); MM(4 * s + 2, 3); SB(); }
    }
    __builtin_amdgcn_s_setprio(2);
    f32x16 y;
    {
      bf16x8 kc[4], vc[4], vb[4][2];
      f32x16 p0, p1, tv[4], tp[2];
      float x[16], e[16], g[16], x1[16], e1[16], g1[16];
      u32x4 fp[2], fm[2];
      MM(30, 0); MM(31, 0); MM(30, 1); MM(31, 1); MM(30, 2); SB();
      kc[0] = cvt8<0>(acc[0]); SB(); MM(31, 2); SB();
      vc[0] = cvt8<8>(acc[0]); SB(); MM(30, 3); SB();
      kc[1] = cvt8<0>(acc[1]); SB(); MM(31, 3); SB();
      vc[1] = cvt8<8>(acc[1]); SB();
      p0 = mfma32(ap[0], kc[0], zero16()); SB(); kc[2] = cvt8<0>(acc[2]); SB();
      p0 = mfma32(ap[1], kc[1], p0); SB(); vc[2] = cvt8<8>(acc[2]); SB();
      tv[0] = mfma32(vc[0], tsel[0], cone); SB(); kc[3] = cvt8<0>(acc[3]); SB();
      tv[1] = mfma32(vc[1], tsel[0], cone); SB(); vc[3] = cvt8<8>(acc[3]); SB();
      p1 = mfma32(ap[0], kc[2], zero16()); SB();
      const float m0 = max16(p0); SB();
      p1 = mfma32(ap[1], kc[3], p1); SB(); vb[0][0] = cvt8<0>(tv[0]); vb[0][1] = cvt8<8>(tv[0]); PIN("+v"(vb[0][0]), "+v"(vb[0][1])); SB();
      tv[2] = mfma32(vc[2], tsel[0], cone); SB(); vb[1][0] = cvt8<0>(tv[1]); vb[1][1] = cvt8<8>(tv[1]); PIN("+v"(vb[1][0]), "+v"(vb[1][1])); SB();
      tv[3] = mfma32(vc[3], tsel[0], cone); SB();
      float m1 = max16(p1); PIN("+v"(m1)); SB();
      vb[2][0] = cvt8<0>(tv[2]); vb[2][1] = cvt8<8>(tv[2]); PIN("+v"(vb[2][0]), "+v"(vb[2][1])); SB();
      args16pk(p0, m0, x); args16pk(p1, m1, x1); SB();
      exp_r<0, 16>(x, e); vb[3][0] = cvt8<0>(tv[3]); vb[3][1] = cvt8<8>(tv[3]); PIN("+v"(vb[3][0]), "+v"(vb[3][1])); rcp_r<0, 16>(e, g); SB();
      float se = sum16pk(e), sg = sum16pk(g); PIN("+v"(se));
      if (UNLIKELY(anyl(!(sg < BIG)))) sg = minus_exact(p0, g);
      scale16pk(e, __builtin_amdgcn_rcpf(se)); scale16pk(g, __builtin_amdgcn_rcpf(sg)); SB();
      STAMP(15);
      pack16(e, fp[0], fp[1]); swap4(fp[0], fp[1]); SB();
      tp[0] = mfma32(asbf(fp[0]), tsel[0], zero16()); SB(); pack16(g, fm[0], fm[1]); SB();
      tp[1] = mfma32(asbf(fp[1]), tsel[0], zero16()); SB(); swap4(fm[0], fm[1]); SB();
      tp[0] = mfma32(asbf(fm[0]), tsel[1], tp[0]); SB(); exp_r<0, 4>(x1, e1); SB();
      tp[1] = mfma32(asbf(fm[1]), tsel[1], tp[1]); SB(); exp_r<4, 16>(x1, e1); SB();
      kc[0] = cvt8<0>(tp[0]); kc[1] = cvt8<8>(tp[0]); SB();
      y = mfma32(kc[0], vb[0][0], zero16()); SB(); kc[2] = cvt8<0>(tp[1]); kc[3] = cvt8<8>(tp[1]); SB();
      y = mfma32(kc[1], vb[0][1], y); SB(); rcp_r<0, 4>(e1, g1); SB();
      y = mfma32(kc[2], vb[1][0], y); SB(); rcp_r<4, 8>(e1, g1); SB();
      y = mfma32(kc[3], vb[1][1], y); SB(); rcp_r<8, 16>(e1, g1); SB();
      se = sum16pk(e1); sg = sum16pk(g1); PIN("+v"(se));
      if (UNLIKELY(anyl(!(sg < BIG)))) sg = minus_exact(p1, g1);
      scale16pk(e1, __builtin_amdgcn_rcpf(se)); scale16pk(g1, __builtin_amdgcn_rcpf(sg)); SB();
      cnt_arrive<2>(cb, one); SB();
      pack16(e1, fp[0], fp[1]); swap4(fp[0], fp[1]); SB();
      tp[0] = mfma32(asbf(fp[0]), tsel[0], zero16()); SB(); pack16(g1, fm[0], fm[1]); SB();
      tp[1] = mfma32(asbf(fp[1]), tsel[0], zero16()); SB(); swap4(fm[0], fm[1]); SB();
      tp[0] = mfma32(asbf(fm[0]), tsel[1], tp[0]);
      tp[1] = mfma32(asbf(fm[1]), tsel[1], tp[1]); SB();
      kc[0] = cvt8<0>(tp[0]); SB();
      y = mfma32(kc[0], vb[2][0], y); SB(); kc[1] = cvt8<8>(tp[0]); SB();
      y = mfma32(kc[1], vb[2][1], y); SB(); kc[2] = cvt8<0>(tp[1]); SB();
      y = mfma32(kc[2], vb[3][0], y); SB(); kc[3] = cvt8<8>(tp[1]); SB();
      y = mfma32(kc[3], vb[3][1], y); SB();
    }
    {
      const u32x4 yp = asu(cvt8<0>(y)), ym = asu(cvt8<8>(y));
      st16o<0>(yp, so0, rsS, kvp_own);
      st16o<512>(ym, so0, rsS, kvp_own);
      const u64 hp = hash4(yp, tag, hc), hm = hash4(ym, tag, hc + 4);
      const u32x4 pr = pair_halves(hp + hm), hz = {(unsigned)hp, (unsigned)(hp >> 32), (unsigned)hm, (unsigned)(hm >> 32)};
      u32x4 cw;
#pragma unroll
      for (int i = 0; i < 4; ++i) { const unsigned zz = (lane & 1) ? ym[i] : yp[i], cc = blk ? hz[i] : pr[i]; cw[i] = (lane & 18) == 16 ? zz : cc; }
      st16o<0>(cw, so2, rsS, kvp_own);
    }
    STAMP(16); STAMPR(54);
    issue_wo(IC(16), IC(23));
    bf16x8 wf[32];
#define RW(f) wf[f] = lds_tr(((f) < 16 ? awo : awo2) + (WO_AT(f) - ((f) < 16 ? 2 * SLOT : L_WO2)), ((f) < 16 ? awo : awo2) + (WO_AT(f) - ((f) < 16 ? 2 * SLOT : L_WO2)) + 2048)
    u32x4 hf;
    asm volatile("buffer_load_dwordx4 %0, %1, %2, 0 offen sc1" : "=v"(hf) : "v"(h1v), "s"(rsS) : "memory");
    cnt_wait<3, 4>(cb);
#pragma unroll
    for (int hd = 0; hd < 16; ++hd) RW(hd);
    cnt_arrive<4>(cb, one);
    STAMP(17);
    unsigned spins = 0;
    WAITV(0);
    PIN("+v"(hf));
    STAMP(18);
    {
      auto late_now = [&]() { const unsigned t = now32() - X1; return (unsigned)(__builtin_amdgcn_ballot_w64(mk64(hf[0], hf[1]) != tag) | __builtin_amdgcn_ballot_w64((int)(hf[2] - t) > 0)) & 0xFFFFu; };
      unsigned late = late_now(), m = M0 == 0 ? (unsigned)(rt >> 3) * 2 : M0 == 1 ? rt & 3 : (rt & 1) * 2;
      if (UNLIKELY(late)) m = ((31 - __builtin_clz(late)) >> 2) ^ 3;
      m = __builtin_amdgcn_readfirstlane(m);
      unsigned qso[4];
#pragma unroll
      for (int i = 0; i < 4; ++i)
        asm("s_xor_b32 %0, %1, %3\n\ts_mul_i32 %0, %0, %4\n\ts_add_u32 %0, %0, %2" : "=&s"(qso[i]) : "s"(m), "s"(kvp_grp), "i"(i), "i"(4 * KVP_STR) : "scc");
      auto gate = [&](unsigned mask) {
        if (UNLIKELY(late & mask)) {
#pragma nounroll
          do {
            if (anyl(mk64(hf[0], hf[1]) != tag))
              asm volatile("buffer_load_dwordx4 %0, %1, %2, 0 offen sc1\n\ts_waitcnt vmcnt(0)" : "+v"(hf) : "v"(h1v), "s"(rsS) : "memory");
            late = late_now();
          } while ((late & mask) && ++spins < SPIN_CAP);
        }
      };
      u32x4 g[4][5], zp;
      u64 zc;
#pragma unroll
      for (int i = 0; i < 4; ++i) asm volatile("" : "=v"(g[i][0]), "=v"(g[i][1]), "=v"(g[i][2]), "=v"(g[i][3]), "=v"(g[i][4]));
      asm volatile("" : "=v"(zp), "=v"(zc));
      gate(15u << (4 * m)); quad_load(g[0], gv, rsS, qso[0]);
      gate(15u << (4 * (1 ^ m))); quad_load(g[1], gv, rsS, qso[1]);
      const u64 tag2 = tag + tag;
      cnt_wait<4, 4>(cb);
      STAMP(19);
      const f32x4 z4 = {0.f, 0.f, 0.f, 0.f};
      f32x4 d[4][2], dz;
      unsigned bad = 0;
      auto sumq = [&](int i) {
        asm volatile("" : "+v"(g[i][0]), "+v"(g[i][1]), "+v"(g[i][2]), "+v"(g[i][3]), "+v"(g[i][4]));
        d[i][0] = mfma16(sel[0], asbf(g[i][0]), z4);
        d[i][1] = mfma16(sel[0], asbf(g[i][2]), z4);
        SB();
        u64 h0 = hash4(g[i][0], tag2, hc), h1 = hash4(g[i][1], tag2, hc);
        PIN("+v"(h0), "+v"(h1));
        SB();
        d[i][0] = mfma16(sel[1], asbf(g[i][1]), d[i][0]);
        d[i][1] = mfma16(sel[1], asbf(g[i][3]), d[i][1]);
        SB();
        h0 = hash4(g[i][2], h0, hc + 4); h1 = hash4(g[i][3], h1, hc + 4);
        if (UNLIKELY(anyl(h0 != mk64(g[i][4][0], g[i][4][1]) || h1 != mk64(g[i][4][2], g[i][4][3])))) { bad |= 1u << i; quad_load(g[i], gv, rsS, qso[i]); }
      };
      auto sumz = [&]() {
        asm volatile("" : "+v"(zp), "+v"(zc));
        dz = mfma16(sel[0], asbf(zp), z4);
        SB();
        if (UNLIKELY(anyl(hash4v(zp, tag, zhc) != zc))) { bad |= 16u; z_load(zp, zc, zv, zcv, rsS, kvp_grp); }
      };
      WAITV(5);
      gate(15u << (4 * (2 ^ m))); quad_load(g[2], gv, rsS, qso[2]);
      sumq(0);
      WAITV(5);
      gate(0xFFFFu); z_load(zp, zc, zv, zcv, rsS, kvp_grp); quad_load(g[3], gv, rsS, qso[3]);
      sumq(1);
      float s0[4], s1[4];
      WAITV(7); sumq(2);
      WAITV(5); sumz();
      WAITV(0); sumq(3);
      if (UNLIKELY(bad)) {
#pragma nounroll
        do {
          WAITV(0);
          const unsigned was = bad; bad = 0;
          if (was & 1) sumq(0);
          if (was & 2) sumq(1);
          if (was & 4) sumq(2);
          if (was & 8) sumq(3);
          if (was & 16) sumz();
        } while (bad && ++spins < SPIN_CAP);
        WAITV(0);
      }
      STAMPV(62, spins);
#pragma unroll
      for (int j = 0; j < 4; ++j) {
        s0[j] = addpp(d[0][0][j], d[1][0][j], d[2][0][j], d[3][0][j]); s1[j] = addpp(d[0][1][j], d[1][1][j], d[2][1][j], d[3][1][j]);
      }
      *(LAS u32x2*)(kw) = u32x2{cvt2(s0[0], s0[1]), cvt2(s0[2], s0[3])};
      *(LAS u32x2*)(kw + 640) = u32x2{cvt2(s1[0], s1[1]), cvt2(s1[2], s1[3])};
      *(LAS u32x2*)(zw) = u32x2{cvt2(dz[0], dz[1]), cvt2(dz[2], dz[3])};
    }
    STAMP(25);
    BAR();
    __builtin_amdgcn_s_setprio(1);
    if (u == 0) {
      const unsigned t = now32();
      st16o<0>(u32x4{(unsigned)tag, (unsigned)(tag >> 32), t, t}, hsv, rsS, hso);
      *(LAS unsigned*)(L + L_TB8) = t;
    }
    issue_wo(IC(23), IC(32));
    STAMP(26);
    unsigned aor0 = L + L_SCR + 2048 + lane * 16, aor = L + lane * 16;
    unsigned ew0 = scr + r32 * 64 + (((2 * h) ^ ((r32 >> 1) & 3)) << 4), ew1 = ew0 ^ 16u, er = scr + (lane >> 2) * 64 + (((lane & 3) ^ ((lane >> 3) & 3)) << 4);
    asm volatile("" : "+v"(aor0), "+v"(aor), "+v"(ew0), "+v"(ew1), "+v"(er));
    BAR();
    f32x16 c2[2] = {zero16(), zero16()};
    bf16x8 ob[64];
#pragma unroll
    for (int i = 0; i < 8; ++i) ob[i] = lds_b128(aor0 + (i & 3) * SCR + (i >> 2) * 1024);
#pragma unroll
    for (int hd = 16; hd < 23; ++hd) RW(hd);
#pragma unroll
    for (int i = 0; i < 8; ++i) c2[i >> 2] = mfma32(wf[i & 3], ob[i], c2[i >> 2]);
    SB();
    WAITV(0);
    cnt_arrive<5>(cb, one);
    cnt_wait<5, 4>(cb);
#pragma unroll
    for (int hd = 23; hd < 32; ++hd) RW(hd);
    STAMP(30);
#define RO(n) do { ob[n] = lds_b128(aor + ((n) - 8) * 1024); } while (0)
#define MO(n) do { c2[((n) >> 2) & 1] = mfma32(wf[4 * ((n) >> 3) + ((n) & 3)], ob[n], c2[((n) >> 2) & 1]); } while (0)
    RAWBAR();
    STAMP(32);
    SB();
#pragma unroll
    for (int i = 8; i < 16; ++i) RO(i);
    SB();
#pragma unroll
    for (int i = 8; i < 16; ++i) { MO(i); SB(); RO(i + 8); SB(); }
#pragma unroll
    for (int i = 16; i < 20; ++i) MO(i);
    SB();
#pragma unroll
    for (int gp = 0; gp < 2; ++gp) {
      RAWBAR();
      SB();
      const int n0 = 24 + 16 * gp;
#pragma unroll
      for (int i = 0; i < 4; ++i) { MO(n0 - 4 + i); SB(); RO(n0 + 2 * i); RO(n0 + 2 * i + 1); SB(); }
#pragma unroll
      for (int i = 0; i < 8; ++i) { MO(n0 + i); SB(); RO(n0 + 8 + i); SB(); }
      if (gp == 0) {
#pragma unroll
        for (int i = 8; i < 12; ++i) MO(n0 + i);
        SB();
      }
    }
    RAWBAR();
    STAMP(38);
    SB();
#pragma unroll
    for (int i = 0; i < 4; ++i) { MO(48 + i); SB(); RO(56 + 2 * i); RO(57 + 2 * i); SB(); }
#pragma unroll
    for (int i = 56; i < 60; ++i) MO(i);
#pragma unroll
    for (int i = 52; i < 55; ++i) MO(i);
    SB();
    auto put = [&](int t) {
      *(LAS bf16x8*)(ew0 + t * 2048) = cvt8<0>(c2[t]);
      *(LAS bf16x8*)(ew1 + t * 2048) = cvt8<8>(c2[t]);
      asm volatile("" ::: "memory");
    };
    auto sto = [&](int i, bf16x8 v) { __builtin_amdgcn_raw_buffer_store_b128(asu(v), rsY, ov, i * 32768, 16); };
    put(0);
    bf16x8 e0 = lds_b128(er), e1 = lds_b128(er + 1024);
    SB();
    MO(55);
#pragma unroll
    for (int i = 60; i < 64; ++i) MO(i);
    SB();
    sto(0, e0); sto(1, e1);
    STAMP(39);
    put(1);
    e0 = lds_b128(er + 2048); e1 = lds_b128(er + 3072);
    sto(2, e0); sto(3, e1);
  }
}

extern "C" void kernel_launch(void* const* d_in, const int* in_sizes, int n_in, void* d_out, int out_size, void* d_ws, size_t ws_size, hipStream_t stream) {
  (void)hipFuncSetAttribute((const void*)hedgehog_fused, hipFuncAttributeMaxDynamicSharedMemorySize, L_BYTES);
  hedgehog_fused<<<256, 512, L_BYTES, stream>>>((const u16*)d_in[0], (const u16*)d_in[1], (const u16*)d_in[2], (const u16*)d_in[3],
                                                (const u16*)d_in[4], (const u16*)d_in[5], (const u16*)d_in[6], (const u16*)d_in[7],
                                                (u16*)d_out, (char*)d_ws);
}
```

```cpp
#include <hip/hip_runtime.h>
#include <hip/hip_bf16.h>
#include <type_traits>
#pragma clang diagnostic ignored "-Wint-to-pointer-cast"

typedef unsigned short u16;
typedef unsigned long long u64;
typedef __bf16 bf16x8 __attribute__((ext_vector_type(8)));
typedef __bf16 bf16x4 __attribute__((ext_vector_type(4)));
typedef __bf16 bf16x2 __attribute__((ext_vector_type(2)));
typedef float f32x16 __attribute__((ext_vector_type(16)));
typedef float f32x4 __attribute__((ext_vector_type(4)));
typedef float f32x2 __attribute__((ext_vector_type(2)));
typedef unsigned u32x4 __attribute__((ext_vector_type(4)));
typedef unsigned u32x2 __attribute__((ext_vector_type(2)));
typedef unsigned long long u64x2 __attribute__((ext_vector_type(2)));
#define LAS __attribute__((address_space(3)))
#define DI __device__ __forceinline__
typedef __amdgpu_buffer_rsrc_t rsrc_t;

#define NB 3
#define XNT 0
#define NB7 7
#define C3AT 12
#define HS2 8
#define QROT 1
#define U2EARLY 0
#define RMIN 60
#define HMARG -100
#define X1 40
#define RFIRST 1
#define XA 95
#define XL 108
#define XB 110
#define XM 135
#define QLA 6
#define WSL 0
#define KP2 2
#define C2EARLY 0
#define HEARLY 1
#define QP 1
#define QSTORE 1
#define HASHM 0
#define CODEPF 1
#define CODE_BYTES 24576
#define HWAVE 1
#define LW1 5
#define NB0 0
#define NBV 0x111111
#define XOFF (2 * (xcd & 3))
#define WQROT 1
#define WOROT 1
#define Q2 7
#define KB 8
#define QX 0
#define QE 8
#define U3LATE 0
#define PACE 0
#define PACE0 0
#define WQD 0
#define PRETOUCH 2
#define HINT2 1
#define STAMPS 0

constexpr unsigned WS_EPOCH = 0;
constexpr unsigned WS_KVP   = 4096;
constexpr unsigned KVH      = 1408;
constexpr unsigned KVP_STR  = 4 * KVH;
constexpr unsigned WS_OCK   = 6u << 20;
constexpr unsigned OCK_STR  = 512;
constexpr unsigned WS_HINT  = WS_OCK + 1024 * 512;
constexpr unsigned WS_HINT1 = WS_HINT + 65536;
constexpr unsigned WS_OF    = 2u << 20;
constexpr unsigned WS_BYTES = 7u << 20;
constexpr unsigned WS_STAMP = 8u << 20;
constexpr unsigned SLOT   = 32768;
constexpr unsigned L_KVF  = 2 * SLOT;
constexpr unsigned L_OWN  = 2 * SLOT + 8192;
constexpr unsigned L_EPI  = 3 * SLOT;
constexpr unsigned L_PHIW = 2 * SLOT + 23 * 4096;
constexpr unsigned L_TAG  = L_PHIW + 2048;
constexpr unsigned L_ZERO = L_TAG + 16;
constexpr unsigned L_CNT  = L_ZERO + 16;
constexpr unsigned L_BYTES = 163840;
constexpr unsigned SPIN_CAP = 1u << 20;
constexpr u64 TAG_STEP = 0x9E3779B97F4A7C15ull;
constexpr unsigned OOB = 0x80000000u;
constexpr float L2E = 1.4426950408889634f;

#define WAITV(n) asm volatile("s_waitcnt vmcnt(" #n ")" ::: "memory")
#define WAITL0() asm volatile("s_waitcnt lgkmcnt(0)" ::: "memory")
#define RAWBAR() do { asm volatile("" ::: "memory"); __builtin_amdgcn_s_barrier(); asm volatile("" ::: "memory"); } while (0)
#define BAR() do { WAITL0(); __builtin_amdgcn_s_barrier(); asm volatile("" ::: "memory"); } while (0)
#define SB() __builtin_amdgcn_sched_barrier(0)
#define PIN(...) asm volatile("" : __VA_ARGS__)
#define UNLIKELY(x) __builtin_expect(!!(x), 0)
#define IC(n) std::integral_constant<int, n>{}
template <int I, int N, class F> DI void sfor(F&& f) { if constexpr (I < N) { f(std::integral_constant<int, I>{}); sfor<I + 1, N>(f); } }

template <int LI, int SI, bool NT = false> DI void dmai(unsigned lds, unsigned voff, rsrc_t rs, unsigned soff) {
  unsigned t;
  if constexpr (NT)
    asm volatile("s_add_u32 m0, %1, %5\n\ts_add_u32 %0, %4, %6\n\tbuffer_load_dwordx4 %2, %3, %0 offen nt lds"
                 : "=&s"(t) : "s"(lds), "v"(voff), "s"(rs), "s"(soff), "i"(LI), "i"(SI) : "memory", "scc");
  else
    asm volatile("s_add_u32 m0, %1, %5\n\ts_add_u32 %0, %4, %6\n\tbuffer_load_dwordx4 %2, %3, %0 offen lds"
                 : "=&s"(t) : "s"(lds), "v"(voff), "s"(rs), "s"(soff), "i"(LI), "i"(SI) : "memory", "scc");
}
template <int LI> DI void unit_load(u64& ck, unsigned lds, unsigned l16, unsigned lck, rsrc_t rs, unsigned so, unsigned sc) {
  asm volatile("s_add_u32 m0, %1, %7\n\ts_nop 0\n\t"
               "buffer_load_dwordx4 %2, %4, %5 offen sc1 lds\n\tbuffer_load_dwordx4 %2, %4, %5 offen offset:1024 sc1 lds\n\t"
               "buffer_load_dwordx4 %2, %4, %5 offen offset:2048 sc1 lds\n\tbuffer_load_dwordx4 %2, %4, %5 offen offset:3072 sc1 lds\n\t"
               "buffer_load_dwordx2 %0, %3, %4, %6 offen sc1"
               : "+v"(ck) : "s"(lds), "v"(l16), "v"(lck), "s"(rs), "s"(so), "s"(sc), "i"(LI) : "memory", "scc");
}
DI void quad_load(u32x4 (&g)[5], unsigned gv, rsrc_t rs, unsigned so) {
  asm volatile("buffer_load_dwordx4 %0, %5, %6, %7 offen sc1\n\tbuffer_load_dwordx4 %2, %5, %6, %7 offen offset:512 sc1\n\t"
               "buffer_load_dwordx4 %1, %5, %6, %7 offen offset:256 sc1\n\tbuffer_load_dwordx4 %3, %5, %6, %7 offen offset:768 sc1\n\t"
               "buffer_load_dwordx4 %4, %5, %6, %7 offen offset:1024 sc1"
               : "+v"(g[0]), "+v"(g[1]), "+v"(g[2]), "+v"(g[3]), "+v"(g[4]) : "v"(gv), "s"(rs), "s"(so) : "memory");
}
DI void z_load(u32x4& z, u64& zc, unsigned zv, unsigned zcv, rsrc_t rs, unsigned so) {
  asm volatile("buffer_load_dwordx4 %0, %2, %4, %5 offen offset:1280 sc1\n\tbuffer_load_dwordx2 %1, %3, %4, %5 offen offset:1312 sc1"
               : "+v"(z), "+v"(zc) : "v"(zv), "v"(zcv), "s"(rs), "s"(so) : "memory");
}
DI unsigned now32() { u64 t; asm volatile("s_memrealtime %0\n\ts_waitcnt lgkmcnt(0)" : "=s"(t) :: "memory"); return (unsigned)t; }
template <int N> DI void waitv() { asm volatile("s_waitcnt vmcnt(%0)" :: "i"(N) : "memory"); }
template <int OFF> DI void st16o(u32x4 v, unsigned voff, rsrc_t rs, unsigned soff) {
  asm volatile("buffer_store_dwordx4 %0, %1, %2, %3 offen offset:%4 sc1\n\ts_nop 1" :: "v"(v), "v"(voff), "s"(rs), "s"(soff), "i"(OFF) : "memory");
}
DI u64 mk64(unsigned lo, unsigned hi) { return ((u64)hi << 32) | lo; }
static constexpr unsigned HC[16] = {};
DI u64 hash4(u32x4 v, u64 s, const unsigned*) { s = (s << 1) + mk64(v[0], v[1]); PIN("+v"(s)); return (s << 1) + mk64(v[2], v[3]); }
DI u64 hash4v(u32x4 v, u64 s, u32x4) { return hash4(v, s, nullptr); }
DI u32x4 pair_halves(u64 c) {
  auto lo = __builtin_amdgcn_permlane32_swap((unsigned)c, (unsigned)c, false, false);
  auto hi = __builtin_amdgcn_permlane32_swap((unsigned)(c >> 32), (unsigned)(c >> 32), false, false);
  return u32x4{lo[0], hi[0], lo[1], hi[1]};
}
DI bool anyl(bool p) { return __builtin_amdgcn_ballot_w64(p) != 0; }

DI bf16x8 lds_b128(unsigned a) { return *(const LAS bf16x8*)a; }
DI bf16x8 lds_tr(unsigned a0, unsigned a1) {
  bf16x4 lo = __builtin_amdgcn_ds_read_tr16_b64_v4bf16((LAS bf16x4*)a0);
  bf16x4 hi = __builtin_amdgcn_ds_read_tr16_b64_v4bf16((LAS bf16x4*)a1);
  return __builtin_shufflevector(lo, hi, 0, 1, 2, 3, 4, 5, 6, 7);
}
DI f32x16 mfma32(bf16x8 a, bf16x8 b, f32x16 c) { return __builtin_amdgcn_mfma_f32_32x32x16_bf16(a, b, c, 0, 0, 0); }
DI f32x4 mfma16(bf16x8 a, bf16x8 b, f32x4 c) { return __builtin_amdgcn_mfma_f32_16x16x32_bf16(a, b, c, 0, 0, 0); }
DI void mfma16i(f32x4& c, bf16x8 a, bf16x8 b) { asm("v_mfma_f32_16x16x32_bf16 %0, %1, %2, %0" : "+v"(c) : "v"(a), "v"(b)); }
DI bf16x8 asbf(u32x4 v) { return __builtin_bit_cast(bf16x8, v); }
DI u32x4 asu(bf16x8 v) { return __builtin_bit_cast(u32x4, v); }
template <int O> DI bf16x8 cvt8(const f32x16& c) {
  bf16x8 r;
#pragma unroll
  for (int i = 0; i < 8; ++i) r[i] = (__bf16)c[O + i];
  return r;
}
DI unsigned cvt2(float a, float b) { bf16x2 v = {(__bf16)a, (__bf16)b}; return __builtin_bit_cast(unsigned, v); }
DI f32x16 zero16() { f32x16 z;
#pragma unroll
  for (int i = 0; i < 16; ++i) z[i] = 0.f;
  return z; }
DI bf16x8 onehot(bool on, int j) {
  const unsigned v = on ? (0x3F80u << (16 * (j & 1))) : 0u;
  return asbf(u32x4{(j >> 1) == 0 ? v : 0u, (j >> 1) == 1 ? v : 0u, (j >> 1) == 2 ? v : 0u, (j >> 1) == 3 ? v : 0u});
}
DI void swap4(u32x4& a, u32x4& c) {
#pragma unroll
  for (int i = 0; i < 4; ++i) { auto r = __builtin_amdgcn_permlane32_swap(a[i], c[i], false, false); a[i] = r[0]; c[i] = r[1]; }
}

DI float max16(const f32x16& p) {
  float m = __builtin_elementwise_maximum(p[0], p[1]);
#pragma unroll
  for (int i = 2; i < 16; i += 2) m = __builtin_elementwise_maximum(__builtin_elementwise_maximum(m, p[i]), p[i + 1]);
  return m;
}
DI float minus_exact(const f32x16& p, float (&g)[16]) {
  float mn = p[0];
#pragma unroll
  for (int i = 1; i < 16; ++i) mn = __builtin_fminf(mn, p[i]);
  float s = 0.f;
#pragma unroll
  for (int i = 0; i < 16; ++i) { g[i] = __builtin_amdgcn_exp2f((mn - p[i]) * L2E); s += g[i]; }
  return s;
}
constexpr float BIG = 0x1p120f;
DI float sum16pk(const float (&e)[16]) {
  f32x2 t[8];
#pragma unroll
  for (int i = 0; i < 8; ++i) t[i] = f32x2{e[2 * i], e[2 * i + 1]};
  const f32x2 s = ((t[0] + t[1]) + (t[2] + t[3])) + ((t[4] + t[5]) + (t[6] + t[7]));
  return s.x + s.y;
}
DI void args16pk(const f32x16& p, float mx, float (&x)[16]) {
  const float a = -mx * L2E;
#pragma unroll
  for (int i = 0; i < 8; ++i) { const f32x2 v = f32x2{p[2 * i], p[2 * i + 1]} * f32x2{L2E, L2E} + f32x2{a, a}; x[2 * i] = v.x; x[2 * i + 1] = v.y; }
}
DI void scale16pk(float (&e)[16], float r) {
#pragma unroll
  for (int i = 0; i < 8; ++i) { const f32x2 v = f32x2{e[2 * i], e[2 * i + 1]} * f32x2{r, r}; e[2 * i] = v.x; e[2 * i + 1] = v.y; }
}
DI void pack16(const float (&e)[16], u32x4& a, u32x4& c) {
#pragma unroll
  for (int i = 0; i < 4; ++i) { a[i] = cvt2(e[2 * i], e[2 * i + 1]); c[i] = cvt2(e[8 + 2 * i], e[9 + 2 * i]); }
}
template <int I0, int I1> DI void exp_r(const float (&x)[16], float (&e)[16]) {
#pragma unroll
  for (int i = I0; i < I1; ++i) e[i] = __builtin_amdgcn_exp2f(x[i]);
}
template <int I0, int I1> DI void rcp_r(const float (&e)[16], float (&g)[16]) {
#pragma unroll
  for (int i = I0; i < I1; ++i) g[i] = __builtin_amdgcn_rcpf(e[i]);
}
DI void fma4(float (&x)[16], int o, const f32x16& p, float c, float a) {
  asm("v_fma_f32 %0, %4, %8, %9\n\tv_fma_f32 %1, %5, %8, %9\n\tv_fma_f32 %2, %6, %8, %9\n\tv_fma_f32 %3, %7, %8, %9"
      : "=&v"(x[o]), "=&v"(x[o + 1]), "=&v"(x[o + 2]), "=&v"(x[o + 3]) : "v"(p[o]), "v"(p[o + 1]), "v"(p[o + 2]), "v"(p[o + 3]), "s"(c), "v"(a));
}
DI float add4(float s, const float (&e)[16], int o) {
  float r;
  asm("v_add_f32_e32 %0, %1, %2\n\tv_add_f32_e32 %0, %0, %3\n\tv_add_f32_e32 %0, %0, %4\n\tv_add_f32_e32 %0, %0, %5"
      : "=&v"(r) : "v"(s), "v"(e[o]), "v"(e[o + 1]), "v"(e[o + 2]), "v"(e[o + 3]));
  return r;
}
DI float add3(float a, float b, float c, float d) {
  float r;
  asm("v_add_f32_e32 %0, %1, %2\n\tv_add_f32_e32 %0, %0, %3\n\tv_add_f32_e32 %0, %0, %4" : "=&v"(r) : "v"(a), "v"(b), "v"(c), "v"(d));
  return r;
}
DI float addpp(float a, float b, float c, float d) {
  float r, t;
  asm("v_add_f32_e32 %0, %2, %3\n\tv_add_f32_e32 %1, %4, %5\n\tv_add_f32_e32 %0, %0, %1" : "=&v"(r), "=&v"(t) : "v"(a), "v"(b), "v"(c), "v"(d));
  return r;
}
DI u32x2 mulcvt4(float a, float b, float c, float d, float r) {
  unsigned x, y; float t0, t1;
  asm("v_mul_f32_e32 %0, %4, %8\n\tv_mul_f32_e32 %2, %5, %8\n\tv_mul_f32_e32 %1, %6, %8\n\tv_mul_f32_e32 %3, %7, %8\n\t"
      "v_cvt_pk_bf16_f32 %0, %0, %2\n\tv_cvt_pk_bf16_f32 %1, %1, %3"
      : "=&v"(x), "=&v"(y), "=&v"(t0), "=&v"(t1) : "v"(a), "v"(b), "v"(c), "v"(d), "v"(r));
  return u32x2{x, y};
}

DI u32x4 mulcvt8(const f32x16& n, float r) {
  u32x4 o; float t0, t1, t2, t3;
  asm("v_mul_f32_e32 %0, %8, %16\n\tv_mul_f32_e32 %4, %9, %16\n\tv_mul_f32_e32 %1, %10, %16\n\tv_mul_f32_e32 %5, %11, %16\n\t"
      "v_mul_f32_e32 %2, %12, %16\n\tv_mul_f32_e32 %6, %13, %16\n\tv_mul_f32_e32 %3, %14, %16\n\tv_mul_f32_e32 %7, %15, %16\n\t"
      "v_cvt_pk_bf16_f32 %0, %0, %4\n\tv_cvt_pk_bf16_f32 %1, %1, %5\n\tv_cvt_pk_bf16_f32 %2, %2, %6\n\tv_cvt_pk_bf16_f32 %3, %3, %7"
      : "=&v"(o[0]), "=&v"(o[1]), "=&v"(o[2]), "=&v"(o[3]), "=&v"(t0), "=&v"(t1), "=&v"(t2), "=&v"(t3)
      : "v"(n[0]), "v"(n[1]), "v"(n[2]), "v"(n[3]), "v"(n[4]), "v"(n[5]), "v"(n[6]), "v"(n[7]), "v"(r));
  return o;
}

#define STAMPF(i, x) do {} while (0)
#define STAMP(i) do {} while (0)
#define STAMPR(i) do {} while (0)
#define STAMPV(i, x) do {} while (0)

template <int C> DI void cnt_arrive(unsigned base, unsigned one) {
  asm volatile("s_mov_b64 exec, 1\n\tds_add_u32 %0, %1 offset:%2\n\ts_mov_b64 exec, -1" :: "v"(base), "v"(one), "i"(4 * C) : "memory");
}
template <int C, int N> DI void cnt_wait(unsigned base) {
  unsigned v, t;
  asm volatile("s_branch 2f\n1:\n\ts_sleep 1\n2:\n\tds_read_b32 %0, %2 offset:%3\n\ts_waitcnt lgkmcnt(0)\n\tv_readfirstlane_b32 %1, %0\n\ts_nop 0\n\ts_cmp_lt_u32 %1, %4\n\ts_cbranch_scc1 1b"
               : "=&v"(v), "=&s"(t) : "v"(base), "i"(4 * C), "i"(N) : "memory", "scc");
}

__global__ __launch_bounds__(512, 2) void hedgehog_fused(
    const u16* xA, const u16* wA, const u16* xB, const u16* wB, const u16* pA, const u16* oA, const u16* pB, const u16* oB,
    u16* out_, char* ws_) {
  extern __shared__ __attribute__((aligned(16))) char smem[];
  const auto ka = __builtin_amdgcn_kernarg_segment_ptr();
  u64x2 axw;
  asm volatile("s_load_dwordx4 %0, %1, %2" : "=s"(axw) : "s"(ka), "s"((blockIdx.x & 4) << 2));
  unsigned cpf;
  {
    u64 code0;
    asm volatile("s_getpc_b64 %0" : "=s"(code0));
    const rsrc_t rsC = __builtin_amdgcn_make_buffer_rsrc((void*)(code0 & ~255ull), 0, CODE_BYTES, 0x00020000);
    if (__builtin_amdgcn_readfirstlane(threadIdx.x) == 0)
      asm volatile("buffer_load_dword %0, %1, %2, %3 offen" : "=v"(cpf) : "v"(threadIdx.x << 12), "s"(rsC), "s"((blockIdx.x >> 3) << 7) : "memory");
  }
  SB();
  STAMPR(60); STAMP(0);
  const unsigned L = (unsigned)(size_t)(LAS char*)smem;
  const int b = blockIdx.x, xcd = b & 7, jj = b >> 3;
#define MAP 1
#define M0 0
  const int br = xcd >> 2;
  const int rt = MAP ? ((xcd >> 1) & 1) * 8 + (jj >> 2) : (xcd & 3) * 4 + (jj >> 3), hg = MAP ? (xcd & 1) * 4 + (jj & 3) : jj & 7;
#define SEGS 0x2222
  const int dir = MAP ? 1 - 2 * (rt & 1) : 1;
  auto chunk_of = [&](int v) -> unsigned {
    int s0 = 0, n = 1;
    for (int d = 7; d >= 0; --d) { const int k = (SEGS >> (4 * d)) & 15; if (k) { if (v < s0 + k) { n = k; break; } s0 += k; } }
    const int r = ((n > 4 ? 2 : 1) * (hg & 3) + (rt & 7)) % n, t = v - s0 + r;
    return (unsigned)(s0 + (t >= n ? t - n : t) + XOFF) & 7;
  };
  const int r3 = (rt & 7) % 3;
  auto prod_of = [&](int st) -> unsigned {
    if (WOROT) { const int k = st - 5 + r3; return (unsigned)(((hg ^ (st + 3)) & 4) | ((hg + ((st & 7) == 0 ? 0 : (st < 5 ? rt + st : 1 + (k >= 3 ? k - 3 : k)))) & 3)); }
    return (unsigned)(((hg ^ (st + 3)) & 4) | ((hg + dir * st) & 3));
  };
  const int tid = threadIdx.x, lane = tid & 63;
  const int w = __builtin_amdgcn_readfirstlane(tid >> 6);
  const int u = w & 3;
  const int r32 = lane & 31, h = lane >> 5, blk = (lane >> 4) & 1, q4 = (lane & 15) >> 2, p4 = lane & 3;
  const unsigned fx = (unsigned)(h ^ ((r32 >> 1) & 7));
  char* ws;
#define MORE_ARGS() \
  u64x2 apo, aow; \
  asm volatile("s_load_dwordx4 %0, %2, %3 offset:0x20\n\ts_load_dwordx4 %1, %2, 0x40\n\ts_waitcnt lgkmcnt(0)" : "=&s"(apo), "=&s"(aow) : "s"(ka), "s"((blockIdx.x & 4) << 2) : "memory"); \
  u16* const out = (u16*)aow[0]; ws = (char*)aow[1];
#define MAKE_WO() \
  rsrc_t rsO = __builtin_amdgcn_make_buffer_rsrc((void*)apo[1], 0, 512 * 1024, 0x00020000); \
  rsrc_t rsS = __builtin_amdgcn_make_buffer_rsrc((void*)ws, 0, WS_BYTES, 0x00020000); \
  PIN("+s"(rsO), "+s"(rsS)); \
  unsigned vwo = (unsigned)((lane >> 4) * 1024 + (hg & 3) * 256 + (((lane & 15) ^ ((lane >> 4) << 2)) << 4)); \
  unsigned cb = L + L_CNT, one = 1; asm volatile("" : "+v"(cb), "+v"(one), "+v"(vwo)); \
  unsigned lwo = L + u * 1024; PIN("+s"(lwo)); \
  auto issue_wo = [&](auto F0, auto F1) {     \
    sfor<decltype(F0)::value, decltype(F1)::value>([&](auto F) { constexpr int f = decltype(F)::value; \
      dmai<WO_AT(f), (f & 3) * 16384>(lwo, vwo, rsO, prod_of(f >> 2) * 65536 + u * 4096); }); \
  };
#define WO_AT(f) ((f) < 23 ? 2 * SLOT + (f) * 4096 : ((f) < 31 ? SLOT + ((f) - 23) * 4096 : 2 * SLOT + 16384))

  if (__builtin_expect(w >= 4, 1)) {
    const unsigned vxe = (unsigned)((lane >> 3) * 1024 + (((lane & 7) ^ (lane >> 4)) << 4));
    const unsigned vxo = vxe ^ 64u;
    const unsigned vwk = (unsigned)((lane >> 3) * 3072 + hg * 128 + (((lane & 7) ^ (((lane >> 4) & 1) << 2)) << 4));
    const unsigned vwv = vwk ^ 32u;
    const unsigned lx = L + u * 4096, lw = L + u * 2048;
    const unsigned sx0 = chunk_of(0) * 128 + (4 * rt + u) * 32768;
    asm volatile("s_waitcnt lgkmcnt(0)" : "+s"(axw) : "v"(vxe), "v"(vxo), "s"(lx), "s"(sx0));
    SB();
    const rsrc_t rsX = __builtin_amdgcn_make_buffer_rsrc((void*)axw[0], 0, 2048 * 1024, 0x00020000);
    const rsrc_t rsW = __builtin_amdgcn_make_buffer_rsrc((void*)axw[1], 0, 512 * 3072, 0x00020000);
    unsigned stab = 0;
    auto issue = [&](auto V, auto I0, auto I1) {
      constexpr int v = decltype(V)::value;
      if constexpr (v < 8) {
        const unsigned c = chunk_of(v);
        const unsigned sx = c * 128 + (4 * rt + u) * 32768, sw = c * (64 * 3072) + u * (16 * 3072);
        sfor<decltype(I0)::value, decltype(I1)::value>([&](auto I) {
          constexpr int i = decltype(I)::value;
          if constexpr (i < 4) dmai<(v & 3) * SLOT + i * 1024, i * 8192, (v < XNT)>(lx, (i & 1) ? vxo : vxe, rsX, sx);
          else if constexpr (i < 6) dmai<(v & 3) * SLOT + 16384 + (i - 4) * 1024, (i - 4) * (8 * 3072) + 1024>(lw, (i & 1) ? vwv : vwk, rsW, sw);
          else dmai<(v & 3) * SLOT + 24576 + (i - 6) * 1024, (i - 6) * (8 * 3072) + 2048>(lw, (i & 1) ? vwv : vwk, rsW, sw);
        });
      } else {
        sfor<decltype(I0)::value, decltype(I1)::value>([&](auto I) {
          constexpr int i = decltype(I)::value;
          const unsigned c = (unsigned)(4 * (v - 8) + (i >> 1) + rt) & 7;
          dmai<(i & 1) * 1024, (i & 1) * (8 * 3072)>(lw + ((stab >> (4 * c)) & 7) * 8192, vwk, rsW, c * (64 * 3072) + u * (16 * 3072));
        });
      }
    };
    STAMP(50);
    issue(IC(0), IC(0), IC(2));
    SB();
    issue(IC(0), IC(2), IC(8)); issue(IC(1), IC(0), IC(3));
    SB();
    MORE_ARGS();
    MAKE_WO();
    u64 tag;
    bf16x8 xf[32], ap[4];
    unsigned axk[2][4];
#pragma unroll
    for (int ks = 0; ks < 4; ++ks) { axk[0][ks] = L + (32 * u + r32) * 128 + ((fx ^ (2 * ks)) << 4); axk[1][ks] = axk[0][ks] + 2 * SLOT; asm volatile("" : "+v"(axk[1][ks])); }
    unsigned aw0 = L + (8 * h + q4) * 128 + (((2 * blk + (p4 & 1)) ^ ((q4 >> 1) << 2)) << 4) + 8 * (p4 >> 1), aw1 = aw0 ^ 64u;
    unsigned apb = L + L_PHIW + (8 * h + q4) * 32 + (2 * blk + (p4 >> 1)) * 8;
    asm volatile("" : "+v"(aw0), "+v"(aw1), "+v"(apb));
    unsigned hc[16];
#pragma unroll
    for (int i = 0; i < 16; ++i) { hc[i] = HC[i]; if (HASHM) asm volatile("" : "+s"(hc[i])); }
    unsigned kb = L + L_KVF + h * 320 + (r32 < 16 ? r32 : 16 + (r32 & 3)) * 16;
    unsigned aown = L + L_OWN + (u & 1) * 4096 + lane * 16;
    unsigned l16 = lane * 16, l16h = h ? OOB : l16, lck = r32 * 16 + h * 8, avf = lx + lane * 16;
    asm volatile("" : "+v"(kb), "+v"(aown), "+v"(l16), "+v"(l16h), "+v"(lck), "+v"(avf));
    unsigned gso[4], gsc[4], hv;
    u64 pre = 0;
    {
      const unsigned tt2 = (unsigned)(br * 64 + 4 * rt + 2 * (hg >> 2));
#pragma unroll
      for (int g = 0; g < 4; ++g) {
        const unsigned pr = prod_of(u + 1 + 4 * (g & 1));
        gso[g] = WS_OF + ((tt2 + (g >> 1)) * 32 + 4 * pr) * 1024; gsc[g] = WS_OCK + ((tt2 + (g >> 1)) * 8 + pr) * OCK_STR;
        asm volatile("" : "+s"(gso[g]), "+s"(gsc[g]));
      }
      hv = WS_HINT + (((unsigned)(br * 16 + rt) * 8 + prod_of(u + 1 + 4 * (lane & 1))) * 2 + (hg >> 2)) * 128;
      PIN("+v"(hv));
    }
    unsigned of_own = WS_OF + (unsigned)(((br * 64 + 4 * rt + u) * 32 + 4 * hg) * 1024), ock = WS_OCK + (unsigned)((br * 64 + 4 * rt + u) * 8 + hg) * OCK_STR;
    unsigned h1v = lane < 8 ? lane * 16 : OOB, h1o = WS_HINT1 + (unsigned)((br * 8 + hg) * 16 + rt) * 128;
    unsigned h2o = WS_HINT + (unsigned)(((br * 16 + rt) * 8 + hg) * 2 + (u >> 1)) * 128;
    asm volatile("" : "+s"(of_own), "+s"(ock), "+v"(h1v), "+s"(h1o), "+s"(h2o));
#pragma unroll
    for (int v = 1; v < 8; ++v) stab |= (unsigned)v << (4 * chunk_of(v));
    PIN("+s"(stab));
    SB();
    issue(IC(1), IC(3), IC(8));
    u64 t7;
    sfor<0, 8>([&](auto S) {
      constexpr int s = decltype(S)::value;
      constexpr int nb = s < 6 ? (NBV >> (4 * (5 - (s < 6 ? s : 0)))) & 15 : (WQROT ? 0 : NB);
      if constexpr (s < 7) { issue(IC(s + 2), IC(0), IC(nb)); waitv<8 + nb + (s == 1)>(); }
      else waitv<NB7>();
      BAR();
      if (s == 0) { STAMP(2); STAMPR(52); } if (s == 7) { STAMP(9); STAMPR(53); }
      if constexpr (s == 7) asm volatile("s_memrealtime %0" : "=s"(t7));
      if constexpr (s == 0) {
        if (u < 2) dmai<L_PHIW, 0>(L + u * 1024, l16, __builtin_amdgcn_make_buffer_rsrc((void*)apo[0], 0, 32 * 512, 0x00020000), (unsigned)(hg * 2048 + u * 1024));
        else
          asm volatile("s_mov_b32 m0, %0\n\ts_nop 0\n\tbuffer_load_dword %1, %2, %3 offen sc1 lds"
                       :: "s"(L + L_TAG), "v"(lane < 2 ? lane * 4 : OOB), "s"(rsS), "s"(WS_EPOCH + br * 128) : "memory");
      }
      if constexpr (s < 6) issue(IC(s + 2), IC(nb), IC(8));
      if constexpr (s == 6) issue(IC(8), IC(nb), IC(NB7));
      if constexpr (s == 7) { if (WQD) __builtin_amdgcn_s_sleep(WQD); issue(IC(8), IC(NB7), IC(8)); issue(IC(9), IC(0), IC(5)); }
      if (s == 2) tag = *(const LAS u64*)(L + L_TAG) + TAG_STEP;
      if constexpr (s >= 2 && s < 6) {
        const bool on = (p4 & 1) == (s & 1);
        ap[s - 2] = lds_tr(on ? apb + (s - 2) * 512 : L + L_ZERO, on ? apb + (s - 2) * 512 + 128 : L + L_ZERO);
      }
#pragma unroll
      for (int ks = 0; ks < 4; ++ks) xf[4 * s + ks] = lds_b128(axk[(s >> 1) & 1][ks] + (s & 1) * SLOT);
      SB();
    });
    issue(IC(9), IC(5), IC(8));
    asm volatile("s_waitcnt lgkmcnt(0)" : "+s"(t7) :: "memory");
    if (u == 3) st16o<0>(u32x4{(unsigned)tag, (unsigned)(tag >> 32), (unsigned)t7, (unsigned)t7}, h1v, rsS, h1o);
    if (u < 3) {
      u64 f;
      asm volatile("buffer_load_dwordx2 %0, %1, %2, %3 offen sc1" : "=v"(f) : "v"(h1v), "s"(rsS), "s"(h2o) : "memory"); pre = f;
    }
    issue_wo(IC(0), IC(8));
    STAMP(10);
    waitv<9>();
    cnt_arrive<1>(cb, one);
    issue_wo(IC(16), IC(16 + Q2));
    waitv<Q2>();
    cnt_arrive<3>(cb, one);
    STAMP(11);
    cnt_wait<1, 4>(cb);
    bf16x8 wq[64];
#define RQ(n) wq[n] = lds_tr(((n) < 32 ? aw0 : aw1) + ((n) & 31) * 2048, ((n) < 32 ? aw0 : aw1) + ((n) & 31) * 2048 + 512)
#pragma unroll
    for (int n = 0; n < QLA; ++n) RQ(n ^ 1);
    cnt_wait<2, 4>(cb);
    __builtin_amdgcn_s_setprio(QP);
    STAMP(12);
    f32x16 q0 = zero16(), q1 = zero16();
    SB();
    sfor<0, 32>([&](auto N) {
      constexpr int n = decltype(N)::value;
      constexpr int e0 = n < 32 - QX ? 0 : n - (32 - QX);
      RQ((n + QLA + e0) ^ 1);
      if constexpr (n >= 32 - QX) RQ((n + QLA + e0 + 1) ^ 1);
      q0 = mfma32(wq[n], xf[n], q0); SB();
      if constexpr (HEARLY == 2 && n == 9) if (u == 2) { waitv<(PRETOUCH != 0)>(); cnt_arrive<11>(cb, one); }
    });
    STAMP(21);
    u32x4 fq[4][2];
    unsigned c10;
    {
      f32x16 p;
      float x[16], e[16], g[16], ca, se, sg, re, rg;
      bf16x8 c0, c1;
      sfor<32, 64>([&](auto N) {
        constexpr int n = decltype(N)::value, i = n - 32;
        constexpr int r0 = 32 + QLA + QX + (i < QE ? 2 * i : QE + i);
        if constexpr (r0 < 64) RQ(r0 ^ 1);
        if constexpr (i < QE && r0 + 1 < 64) RQ((r0 + 1) ^ 1);
        q1 = mfma32(wq[n], xf[i], q1);
        SB();
        if constexpr (i == 16) STAMP(24);
        if constexpr (i == 2) c0 = cvt8<0>(q0);
        if constexpr (i == 3) { c1 = cvt8<8>(q0); p = mfma32(ap[0], c0, zero16()); }
        if constexpr (i == 4) p = mfma32(ap[1], c1, p);
        if constexpr (i == 8) ca = -max16(p) * L2E;
        if constexpr (i == 9) { fma4(x, 0, p, L2E, ca); fma4(x, 4, p, L2E, ca); }
        if constexpr (i == 10) { exp_r<0, 3>(x, e); }
        if constexpr (i == 11) { fma4(x, 8, p, L2E, ca); exp_r<3, 5>(x, e); }
        if constexpr (i == 12) { fma4(x, 12, p, L2E, ca); exp_r<5, 7>(x, e); }
        if constexpr (i >= 13 && i < 16) exp_r<7 + 3 * (i - 13), 10 + 3 * (i - 13)>(x, e);
        if constexpr (i == 16) { rcp_r<0, 2>(e, g); se = add3(e[0], e[1], e[2], e[3]); }
        if constexpr (i >= 17 && i < 20) { rcp_r<2 * (i - 16), 2 * (i - 15)>(e, g); se = add4(se, e, 4 * (i - 16)); }
        if constexpr (i == 20) { re = __builtin_amdgcn_rcpf(se); PIN("+v"(re)); rcp_r<8, 10>(e, g); sg = add3(g[0], g[1], g[2], g[3]); }
        if constexpr (i == 21) { rcp_r<10, 12>(e, g); sg = add4(sg, g, 4); }
        if constexpr (i == 22) { rcp_r<12, 16>(e, g); }
        if constexpr (i == 23) { sg = add4(sg, g, 8); sg = add4(sg, g, 12); }
        if constexpr (i == 24) { if (UNLIKELY(anyl(!(sg < BIG)))) sg = minus_exact(p, g); rg = __builtin_amdgcn_rcpf(sg); }
        if constexpr (i >= 24 && i < 28) { constexpr int k = i - 24; const u32x2 t = mulcvt4(e[4 * k], e[4 * k + 1], e[4 * k + 2], e[4 * k + 3], re); fq[k >> 1][0][2 * (k & 1)] = t.x; fq[k >> 1][0][2 * (k & 1) + 1] = t.y; }
        if constexpr (i >= 28) { constexpr int k = i - 28; const u32x2 t = mulcvt4(g[4 * k], g[4 * k + 1], g[4 * k + 2], g[4 * k + 3], rg); fq[k >> 1][1][2 * (k & 1)] = t.x; fq[k >> 1][1][2 * (k & 1) + 1] = t.y; }
        SB();
      });
      STAMP(13);
      WAITV(0);
      asm volatile("" :: "v"(pre));
      cnt_arrive<9>(cb, one);
      asm volatile("s_nop 1" : "+v"(fq[0][0]), "+v"(fq[1][0]), "+v"(fq[0][1]), "+v"(fq[1][1]));
      swap4(fq[0][0], fq[1][0]);
      SB();
      c0 = cvt8<0>(q1); c1 = cvt8<8>(q1);
      p = mfma32(ap[2], c0, zero16());
      p = mfma32(ap[3], c1, p);
      SB();
      swap4(fq[0][1], fq[1][1]);
      SB();
      args16pk(p, max16(p), x);
      exp_r<0, 16>(x, e); rcp_r<0, 16>(e, g);
      se = sum16pk(e); sg = sum16pk(g); PIN("+v"(se));
      if (UNLIKELY(anyl(!(sg < BIG)))) sg = minus_exact(p, g);
      scale16pk(e, __builtin_amdgcn_rcpf(se)); scale16pk(g, __builtin_amdgcn_rcpf(sg));
      pack16(e, fq[2][0], fq[3][0]); pack16(g, fq[2][1], fq[3][1]);
      c10 = *(volatile LAS unsigned*)(L + L_CNT + 40);
      SB();
      swap4(fq[2][0], fq[3][0]); swap4(fq[2][1], fq[3][1]);
    }
#pragma unroll
    for (int a = 0; a < 4; ++a) asm volatile("" : "+v"(fq[a][0]), "+v"(fq[a][1]));
    __builtin_amdgcn_s_setprio(3);
    STAMP(14);
    if ((unsigned)__builtin_amdgcn_readfirstlane(c10) < 4) cnt_wait<10, 4>(cb);
    asm volatile("" ::: "memory");
    if ((u & 1) == HWAVE) { const unsigned t = now32(); st16o<0>(u32x4{(unsigned)tag, (unsigned)(tag >> 32), t, t}, h1v, rsS, h2o); }
    STAMP(26); STAMPR(55);
    u32x4 hf;
    {
      bf16x8 ka[4][2];
#pragma unroll
      for (int a = 0; a < 4; ++a) { ka[a][0] = lds_b128(kb + a * 2048); ka[a][1] = lds_b128(kb + a * 2048 + 640); }
      SB();
      f32x16 n[4];
      u32x4 ou[4];
      u64 hs = tag;
      float rd[4];
      const bool first = (u & 1) == 0;
      auto m0 = [&](int a) { n[a] = mfma32(ka[a][0], asbf(fq[a][0]), zero16()); SB(); };
      auto m1 = [&](int a) { n[a] = mfma32(ka[a][1], asbf(fq[a][1]), n[a]); SB(); };
      auto den = [&](int a) { rd[a] = __builtin_amdgcn_rcpf((((n[a][8] + n[a][9]) + n[a][10]) + n[a][11]) + 1e-6f); PIN("+v"(rd[a])); SB(); };
      auto half = [&](int a, int k) { const u32x2 t = mulcvt4(n[a][4 * k], n[a][4 * k + 1], n[a][4 * k + 2], n[a][4 * k + 3], rd[a]); ou[a][2 * k] = t.x; ou[a][2 * k + 1] = t.y; SB(); };
      auto finpk = [&](int a) {
#pragma unroll
        for (int i = 0; i < 4; ++i) { const f32x2 v = f32x2{n[a][2 * i], n[a][2 * i + 1]} * f32x2{rd[a], rd[a]}; ou[a][i] = cvt2(v.x, v.y); }
      };
      auto sto = [&](auto A) { constexpr int a = decltype(A)::value; if (__builtin_expect(first, 1)) st16o<a * 1024>(ou[a], l16, rsS, of_own); SB(); };
      m0(0); m1(0); m0(1); m1(1); m0(2);
      den(0); m1(2);
      half(0, 0); m0(3);
      half(0, 1); m1(3);
      sto(IC(0)); den(1); den(2);
      finpk(1); sto(IC(1)); den(3);
      finpk(2); sto(IC(2));
      finpk(3); sto(IC(3));
      hs = hash4(ou[3], hash4(ou[2], hash4(ou[1], hash4(ou[0], hs, hc), hc + 4), hc + 8), hc + 12);
      asm volatile("" :: "v"(n[0]), "v"(n[1]), "v"(n[2]), "v"(n[3]));
      u32x4 cw = pair_halves(hs);
      PIN("+v"(cw));
      if (__builtin_expect(first, 1)) st16o<0>(cw, l16h, rsS, ock);
      asm volatile("buffer_load_dwordx4 %0, %1, %2, 0 offen sc1" : "=v"(hf) : "v"(hv), "s"(rsS) : "memory");
      STAMP(27);
      RAWBAR();
      if (!first) {
        st16o<0>(ou[0], l16, rsS, of_own); st16o<1024>(ou[1], l16, rsS, of_own); st16o<2048>(ou[2], l16, rsS, of_own); st16o<3072>(ou[3], l16, rsS, of_own);
        st16o<0>(cw, l16h, rsS, ock);
      }
      if ((u >> 1) == (hg >> 2)) {
#pragma unroll
        for (int a = 0; a < 4; ++a) *(LAS u32x4*)(aown + a * 1024) = ou[a];
        cnt_arrive<6>(cb, one);
      }
      STAMP(28);
      if (first) waitv<0>(); else waitv<5>();
    }
    unsigned spins2 = 0, fails = 0;
    PIN("+v"(hf));
    auto until = [&](unsigned par, unsigned x) {
      const u64 lanes = 0x5555555555555555ull << par;
      if (UNLIKELY(__builtin_amdgcn_ballot_w64(mk64(hf[0], hf[1]) != tag) & lanes)) {
#pragma nounroll
        do asm volatile("buffer_load_dwordx4 %0, %1, %2, 0 offen sc1\n\ts_waitcnt vmcnt(0)" : "+v"(hf) : "v"(hv), "s"(rsS) : "memory");
        while ((__builtin_amdgcn_ballot_w64(mk64(hf[0], hf[1]) != tag) & lanes) && ++spins2 < SPIN_CAP);
      }
      const unsigned t = __builtin_amdgcn_readlane(hf[2], par) + x;
#pragma nounroll
      for (int k = 0; k < 64 && (int)(t - now32()) > 0; ++k) {}
    };
    until(0, XA);
    STAMP(29);
    u64 ck[4];
    asm volatile("" : "=v"(ck[0]), "=v"(ck[1]), "=v"(ck[2]), "=v"(ck[3]));
    auto verify = [&](auto G) {
      constexpr int g = decltype(G)::value;
      auto bad = [&]() {
        asm volatile("" : "+v"(ck[g]) :: "memory");
        const u32x4 v0 = *(const LAS u32x4*)(avf + g * 16384), v1 = *(const LAS u32x4*)(avf + g * 16384 + 1024);
        const u32x4 v2 = *(const LAS u32x4*)(avf + g * 16384 + 2048), v3 = *(const LAS u32x4*)(avf + g * 16384 + 3072);
        return anyl(hash4(v3, hash4(v2, hash4(v1, hash4(v0, tag, hc), hc + 4), hc + 8), hc + 12) != ck[g]);
      };
      if (UNLIKELY(bad())) {
#pragma nounroll
        do { unit_load<g * 16384>(ck[g], lx, l16, lck, rsS, gso[g], gsc[g]); WAITV(0); } while (++spins2 < SPIN_CAP && bad());
      }
    };
    unit_load<0>(ck[0], lx, l16, lck, rsS, gso[0], gsc[0]);
    if (u < 3) { until(1, XL); unit_load<16384>(ck[1], lx, l16, lck, rsS, gso[1], gsc[1]); }
    STAMP(30);
    if (u < 3) waitv<5>(); else waitv<0>();
    STAMP(31);
    verify(IC(0));
    RAWBAR();
    STAMP(32); STAMPR(56);
    unit_load<32768>(ck[2], lx, l16, lck, rsS, gso[2], gsc[2]);
    if (u < 3) { waitv<5>(); verify(IC(1)); }
    if (u < 3) unit_load<49152>(ck[3], lx, l16, lck, rsS, gso[3], gsc[3]);
    STAMP(33);
    RAWBAR();
    if (u < 3) waitv<5>(); else waitv<0>();
    verify(IC(2));
    STAMP(34);
    RAWBAR();
    if (u < 3) { waitv<0>(); verify(IC(3)); }
    STAMP(35);
    RAWBAR();
    STAMP(38);
    {
      const rsrc_t rsY = __builtin_amdgcn_make_buffer_rsrc((void*)(out + (size_t)(rt * 128 + (hg >> 2) * 64) * 1024 + br * 512 + (hg & 3) * 128 + u * 32), 0, 63 * 2048 + 64, 0x00020000);
      const unsigned er = L + L_EPI + u * 4096 + (lane >> 2) * 64 + (((lane & 3) ^ ((lane >> 3) & 3)) << 4), ov = (unsigned)((lane >> 2) * 2048 + (lane & 3) * 16);
      cnt_wait<8, 4>(cb);
      const bf16x8 e0 = lds_b128(er), e1 = lds_b128(er + 1024);
      __builtin_amdgcn_raw_buffer_store_b128(asu(e0), rsY, ov, 0, 16);
      __builtin_amdgcn_raw_buffer_store_b128(asu(e1), rsY, ov, 32768, 16);
    }
    STAMPF(63, fails);
    if (rt == 0 && hg == 0 && u == 0)
      asm volatile("buffer_store_dwordx2 %0, %1, %2, %3 offen sc1" :: "v"(tag), "v"(lane == 0 ? 0u : OOB), "s"(rsS), "s"(WS_EPOCH + br * 128) : "memory");
  } else {
    u64x2 apo, aow;
    asm volatile("s_load_dwordx4 %0, %2, %3 offset:0x20\n\ts_load_dwordx4 %1, %2, 0x40" : "=&s"(apo), "=&s"(aow) : "s"(ka), "s"((blockIdx.x & 4) << 2));
    SB();
    f32x4 ak[8], av[8];
#pragma unroll
    for (int t = 0; t < 8; ++t) { ak[t] = f32x4{0.f, 0.f, 0.f, 0.f}; av[t] = ak[t]; }
    unsigned ax0 = (lane & 15) * 128 + ((((lane >> 4)) ^ ((lane >> 1) & 7)) << 4), ax1 = ax0 ^ 64u;
    unsigned awb = 16384 + (8 * (lane >> 4) + q4) * 128 + (((2 * u + (p4 >> 1)) ^ ((q4 >> 1) << 2) ^ (blk << 1)) << 4) + 8 * (p4 & 1);
    PIN("+v"(ax0), "+v"(ax1), "+v"(awb));
    const unsigned scr = L + L_KVF + u * 2048;
    const int jt = (lane & 3) + 4 * ((lane >> 3) & 1);
    unsigned so0, so2, apa[2][2];
    {
      const unsigned dcol = 8 * ((lane >> 2) & 1) + jt;
      so0 = blk == 0 ? h * 256 + dcol * 16 : OOB;
      so2 = blk == 0 ? (h ? OOB : 1024 + dcol * 16) : ((lane & 15) < 4 ? 1280u + h * 64 + p4 * 16 : OOB);
      asm volatile("" : "+v"(so0), "+v"(so2));
#pragma unroll
      for (int s = 0; s < 2; ++s) {
        const unsigned base = L + L_PHIW + u * 512 + (8 * h + q4) * 32 + (2 * blk + (p4 >> 1)) * 8;
        const bool on = (p4 & 1) == s;
        apa[s][0] = on ? base : L + L_ZERO; apa[s][1] = on ? base + 128 : L + L_ZERO;
        asm volatile("" : "+v"(apa[s][0]), "+v"(apa[s][1]));
      }
    }
    bf16x8 tsel[2] = {onehot(((lane >> 2) & 1) == h && blk == 0, jt), onehot(((lane >> 2) & 1) == h && blk == 1, jt)};
    bf16x8 sel[2] = {onehot(((lane >> 3) & 1) == 0, lane & 7), onehot(((lane >> 3) & 1) == 1, lane & 7)};
    f32x16 cone;
#pragma unroll
    for (int i = 0; i < 16; ++i) cone[i] = blk ? 1.f : 0.f;
    asm volatile("" : "+v"(tsel[0]), "+v"(tsel[1]), "+v"(sel[0]), "+v"(sel[1]), "+v"(cone));
    unsigned awo = L + SLOT + (4 * h + q4) * 256 + (((4 * u + 2 * (p4 & 1) + blk) ^ (q4 << 2)) << 4) + 8 * (p4 >> 1);
    unsigned awo2 = awo + 2 * SLOT; PIN("+v"(awo2));
    unsigned gv = (unsigned)((lane >> 4) * KVP_STR + (lane & 15) * 16);
    unsigned zv = (unsigned)((lane >> 2) * KVP_STR + ((lane >> 1) & 1) * 64 + (lane & 1) * 16), zcv = zv - (lane & 1) * 8;
    unsigned kw = scr + h * 320 + (lane & 15) * 16 + blk * 8;
    unsigned zw = h ? scr + 1280 + lane * 8 : scr + (2 * (lane & 1) + ((lane >> 1) & 1)) * 320 + 256 + ((lane >> 2) & 3) * 16 + blk * 8;
    unsigned ov = (unsigned)((lane >> 2) * 2048 + (lane & 3) * 16);
    asm volatile("" : "+v"(awo), "+v"(gv), "+v"(zv), "+v"(zcv), "+v"(kw), "+v"(zw), "+v"(ov));
    unsigned kvp_grp = WS_KVP + (unsigned)((br * 8 + hg) * 16) * KVP_STR + u * KVH, kvp_own = kvp_grp + rt * KVP_STR;
    unsigned hc[8], h1v = WS_HINT1 + (unsigned)((br * 8 + hg) * 16 + (lane & 15)) * 128;
    PIN("+v"(h1v));
#pragma unroll
    for (int i = 0; i < 8; ++i) { hc[i] = HC[i]; if (HASHM) asm volatile("" : "+s"(hc[i])); }
    u32x4 zhc;
#pragma unroll
    for (int i = 0; i < 4; ++i) zhc[i] = HC[i] + (unsigned)(lane & 1) * (HC[4 + i] - HC[i]);
    asm volatile("" : "+s"(kvp_grp), "+s"(kvp_own), "+v"(zhc));
    u64 tag;
    bf16x8 wk[2], wv[2], xb[2][8], ap[2];
#define MKB(k2, t) do { ak[t] = mfma16(wk[k2], xb[k2][t], ak[t]); SB(); } while (0)
#define MVB(k2, t) do { av[t] = mfma16(wv[k2], xb[k2][t], av[t]); SB(); } while (0)
#define MKI(k2, t) do { mfma16i(ak[t], wk[k2], xb[k2][t]); SB(); } while (0)
#define MVI(k2, t) do { mfma16i(av[t], wv[k2], xb[k2][t]); SB(); } while (0)
#define RK(k2) do { wk[k2] = lds_tr(awb + (k2) * 4096, awb + (k2) * 4096 + 512); SB(); } while (0)
#define RV(k2) do { wv[k2] = lds_tr(awb + 8192 + (k2) * 4096, awb + 8192 + (k2) * 4096 + 512); SB(); } while (0)
#define RX(k2, t) do { xb[k2][t] = lds_b128(((k2) ? ax1 : ax0) + (t) * 2048); SB(); } while (0)
#define NEXT(a) do { a = (a + SLOT) & (4 * SLOT - 1); SB(); } while (0)
#define HEAD() do { \
      MKI(1, 3); RK(0); MVI(1, 3); RV(0); MKI(1, 4); RX(0, 1); MVI(1, 4); RX(0, 0); MKI(1, 5); RX(0, 3); MVI(1, 5); RX(0, 2); \
      MKI(1, 6); RX(0, 5); MVI(1, 6); RX(0, 4); MKI(1, 7); RX(0, 7); MVI(1, 7); RX(0, 6); } while (0)
    {
      const bf16x8 z8 = asbf(u32x4{0, 0, 0, 0});
      wk[1] = z8; wv[1] = z8;
#pragma unroll
      for (int t = 3; t < 8; ++t) xb[1][t] = z8;
      PIN("+v"(wk[1]), "+v"(wv[1]), "+v"(xb[1][3]), "+v"(xb[1][4]), "+v"(xb[1][5]), "+v"(xb[1][6]), "+v"(xb[1][7]));
#pragma unroll
      for (int t = 0; t < 8; ++t) PIN("+v"(ak[t]), "+v"(av[t]));
    }
    SB();
    asm volatile("s_waitcnt lgkmcnt(0)" : "+s"(apo), "+s"(aow), "+s"(axw));
    u16* const out = (u16*)aow[0]; ws = (char*)aow[1];
    MAKE_WO();
    rsrc_t rsY = __builtin_amdgcn_make_buffer_rsrc((void*)(out + (size_t)(rt * 128 + (hg >> 2) * 64) * 1024 + br * 512 + (hg & 3) * 128 + u * 32), 0, 63 * 2048 + 64, 0x00020000);
    PIN("+s"(rsY));
    STAMP(1);
#pragma nounroll
    for (int s = 0; s < 7; ++s) {
      BAR();
      SB();
      HEAD();
      MKI(0, 0); RK(1); MVI(0, 0); RV(1); MKI(0, 1); RX(1, 1); MVI(0, 1); RX(1, 0);
      MKI(0, 2); RX(1, 3); MVI(0, 2); RX(1, 2); MKI(0, 3); RX(1, 5); MVI(0, 3); RX(1, 4);
      MKI(0, 4); RX(1, 7); MVI(0, 4); RX(1, 6); MKI(0, 5); NEXT(ax0); MVI(0, 5); NEXT(ax1); MKI(0, 6); NEXT(awb);
      MVI(0, 6); MKI(0, 7); MVI(0, 7);
      MKI(1, 0); MVI(1, 0); MKI(1, 1); MVI(1, 1); MKI(1, 2); MVI(1, 2);
      if (PACE && s >= PACE0) __builtin_amdgcn_s_sleep(PACE);
    }
    WAITV(0);
    asm volatile("" :: "v"(cpf));
    BAR();
    STAMP(9);
    SB();
    HEAD();
    MKI(0, 0); RK(1); MKI(0, 1); RX(1, 1); MVI(0, 0); RX(1, 0); MKI(0, 2); RX(1, 3); MKI(0, 3); RX(1, 2);
    MVI(0, 1); RX(1, 5); MKI(0, 4); RX(1, 4); MKI(0, 5); RX(1, 7); MKI(0, 6); RX(1, 6); MKI(0, 7); RV(1);
    MKB(1, 0); ap[0] = lds_tr(apa[0][0], apa[0][1]); SB();
    MKB(1, 1); ap[1] = lds_tr(apa[1][0], apa[1][1]); SB();
    MKB(1, 2); tag = *(const LAS u64*)(cb - (L_CNT - L_TAG)); SB();
    MKB(1, 3);
    tag += TAG_STEP;
    __builtin_amdgcn_s_setprio(2);
    f32x16 y;
    u32x4 hf;
    {
      bf16x8 kc[4], vb[4][2];
      f32x16 p0, p1, tv[4], tp[2];
      float x[16], e[16], g[16], x1[16], e1[16], g1[16];
      u32x4 fp[2], fm[2];
      u32x4 kd[4], vd[4];
#define CV(dst, src, t) do { dst[(t) >> 1][2 * ((t) & 1)] = cvt2(src[t][0], src[t][1]); dst[(t) >> 1][2 * ((t) & 1) + 1] = cvt2(src[t][2], src[t][3]); SB(); } while (0)
#define SW(dst, T, i) do { auto r_ = __builtin_amdgcn_permlane16_swap(dst[T][i], dst[T][2 + (i)], false, false); dst[T][i] = r_[0]; dst[T][2 + (i)] = r_[1]; SB(); } while (0)
      MKB(1, 4); MKB(1, 5); MKB(1, 6); MKB(1, 7);
      MVI(0, 2); CV(kd, ak, 0); MVI(0, 3); CV(kd, ak, 1); MVB(1, 0); CV(kd, ak, 2); MVB(1, 1); CV(kd, ak, 3);
      MVB(1, 2); SW(kd, 0, 0); MVB(1, 3); SW(kd, 0, 1); MVI(0, 4); SW(kd, 1, 0); MVI(0, 5); SW(kd, 1, 1);
      MVI(0, 6); CV(kd, ak, 4); MVI(0, 7); CV(kd, ak, 5); MVB(1, 4); CV(kd, ak, 6); MVB(1, 5); CV(kd, ak, 7);
      p0 = mfma32(ap[0], asbf(kd[0]), zero16()); SB();
      SW(kd, 2, 0); SW(kd, 2, 1); CV(vd, av, 0);
      p0 = mfma32(ap[1], asbf(kd[1]), p0); SB(); SW(kd, 3, 0); SW(kd, 3, 1); CV(vd, av, 1);
      MVB(1, 6); CV(vd, av, 2); MVB(1, 7); CV(vd, av, 3);
      p1 = mfma32(ap[0], asbf(kd[2]), zero16()); SB(); SW(vd, 0, 0); SW(vd, 0, 1); CV(vd, av, 4);
      p1 = mfma32(ap[1], asbf(kd[3]), p1); SB(); SW(vd, 1, 0); SW(vd, 1, 1); CV(vd, av, 5);
      tv[0] = mfma32(asbf(vd[0]), tsel[0], cone); SB(); CV(vd, av, 6); CV(vd, av, 7); SW(vd, 2, 0); SW(vd, 2, 1);
      tv[1] = mfma32(asbf(vd[1]), tsel[0], cone); SB(); SW(vd, 3, 0); SW(vd, 3, 1);
      const float m0 = max16(p0); SB();
      tv[2] = mfma32(asbf(vd[2]), tsel[0], cone); SB();
      float m1 = max16(p1); PIN("+v"(m1)); SB();
      tv[3] = mfma32(asbf(vd[3]), tsel[0], cone); SB();
      vb[0][0] = cvt8<0>(tv[0]); vb[0][1] = cvt8<8>(tv[0]); PIN("+v"(vb[0][0]), "+v"(vb[0][1])); SB();
      vb[1][0] = cvt8<0>(tv[1]); vb[1][1] = cvt8<8>(tv[1]); PIN("+v"(vb[1][0]), "+v"(vb[1][1])); SB();
      vb[2][0] = cvt8<0>(tv[2]); vb[2][1] = cvt8<8>(tv[2]); PIN("+v"(vb[2][0]), "+v"(vb[2][1])); SB();
      args16pk(p0, m0, x); args16pk(p1, m1, x1); SB();
      exp_r<0, 16>(x, e); vb[3][0] = cvt8<0>(tv[3]); vb[3][1] = cvt8<8>(tv[3]); PIN("+v"(vb[3][0]), "+v"(vb[3][1])); rcp_r<0, 16>(e, g); SB();
      float se = sum16pk(e), sg = sum16pk(g); PIN("+v"(se));
      if (UNLIKELY(anyl(!(sg < BIG)))) sg = minus_exact(p0, g);
      scale16pk(e, __builtin_amdgcn_rcpf(se)); scale16pk(g, __builtin_amdgcn_rcpf(sg)); SB();
      STAMP(15);
      pack16(e, fp[0], fp[1]); swap4(fp[0], fp[1]); SB();
      tp[0] = mfma32(asbf(fp[0]), tsel[0], zero16()); SB(); pack16(g, fm[0], fm[1]); SB();
      tp[1] = mfma32(asbf(fp[1]), tsel[0], zero16()); SB(); swap4(fm[0], fm[1]); SB();
      tp[0] = mfma32(asbf(fm[0]), tsel[1], tp[0]); SB(); exp_r<0, 4>(x1, e1); SB();
      tp[1] = mfma32(asbf(fm[1]), tsel[1], tp[1]); SB(); exp_r<4, 16>(x1, e1); SB();
      kc[0] = cvt8<0>(tp[0]); kc[1] = cvt8<8>(tp[0]); SB();
      y = mfma32(kc[0], vb[0][0], zero16()); SB(); kc[2] = cvt8<0>(tp[1]); kc[3] = cvt8<8>(tp[1]); SB();
      y = mfma32(kc[1], vb[0][1], y); SB(); rcp_r<0, 4>(e1, g1); SB();
      y = mfma32(kc[2], vb[1][0], y); SB(); rcp_r<4, 8>(e1, g1); SB();
      y = mfma32(kc[3], vb[1][1], y); SB(); rcp_r<8, 16>(e1, g1); SB();
      se = sum16pk(e1); sg = sum16pk(g1); PIN("+v"(se));
      if (UNLIKELY(anyl(!(sg < BIG)))) sg = minus_exact(p1, g1);
      scale16pk(e1, __builtin_amdgcn_rcpf(se)); scale16pk(g1, __builtin_amdgcn_rcpf(sg)); SB();
      cnt_arrive<2>(cb, one); SB();
      pack16(e1, fp[0], fp[1]); swap4(fp[0], fp[1]); SB();
      tp[0] = mfma32(asbf(fp[0]), tsel[0], zero16()); SB(); pack16(g1, fm[0], fm[1]); SB();
      tp[1] = mfma32(asbf(fp[1]), tsel[0], zero16()); SB(); swap4(fm[0], fm[1]); SB();
      asm volatile("buffer_load_dwordx4 %0, %1, %2, 0 offen sc1" : "=v"(hf) : "v"(h1v), "s"(rsS) : "memory");
      tp[0] = mfma32(asbf(fm[0]), tsel[1], tp[0]);
      tp[1] = mfma32(asbf(fm[1]), tsel[1], tp[1]); SB();
      kc[0] = cvt8<0>(tp[0]); SB();
      y = mfma32(kc[0], vb[2][0], y); SB(); kc[1] = cvt8<8>(tp[0]); SB();
      y = mfma32(kc[1], vb[2][1], y); SB(); kc[2] = cvt8<0>(tp[1]); SB();
      y = mfma32(kc[2], vb[3][0], y); SB(); kc[3] = cvt8<8>(tp[1]); SB();
      y = mfma32(kc[3], vb[3][1], y); SB();
    }
    {
      const u32x4 yp = asu(cvt8<0>(y)), ym = asu(cvt8<8>(y));
      st16o<0>(yp, so0, rsS, kvp_own);
      st16o<512>(ym, so0, rsS, kvp_own);
      const u64 hp = hash4(yp, tag, hc), hm = hash4(ym, tag, hc + 4);
      const u32x4 pr = pair_halves(HASHM ? hp + hm : hash4(ym, hp, hc)), hz = {(unsigned)hp, (unsigned)(hp >> 32), (unsigned)hm, (unsigned)(hm >> 32)};
      u32x4 cw;
#pragma unroll
      for (int i = 0; i < 4; ++i) { const unsigned zz = (lane & 1) ? ym[i] : yp[i], cc = blk ? hz[i] : pr[i]; cw[i] = (lane & 18) == 16 ? zz : cc; }
      st16o<0>(cw, so2, rsS, kvp_own);
    }
    STAMP(16); STAMPR(54);
    const unsigned tP = now32();
    __builtin_amdgcn_s_setprio(KP2);
    cnt_wait<1, 4>(cb); cnt_wait<2, 4>(cb);
    issue_wo(IC(8), IC(8 + KB));
    bf16x8 wf[32];
#define RWA(f) (WO_AT(f) < 3 * SLOT ? awo + (WO_AT(f) - SLOT) : awo2 + (WO_AT(f) - 3 * SLOT))
#define RW(f) wf[f] = lds_tr(RWA(f), RWA(f) + 2048)
    auto wo_a = [&]() {
      cnt_wait<3, 4>(cb);
#pragma unroll
      for (int hd = 0; hd < 5; ++hd) RW(hd);
      cnt_arrive<4>(cb, one);
    };
    unsigned spins = 0;
    waitv<3 + KB>();
    PIN("+v"(hf));
    STAMP(18);
    {
      const unsigned span = min(tP - (unsigned)__builtin_amdgcn_readlane(hf[2], rt), 400u) + X1;
      auto late_now = [&]() { const unsigned t = now32() - span; return (unsigned)(__builtin_amdgcn_ballot_w64(mk64(hf[0], hf[1]) != tag) | __builtin_amdgcn_ballot_w64((int)(hf[2] - t) > 0)) & 0xFFFFu; };
      unsigned late = late_now(), m = M0 == 0 ? (unsigned)(rt >> 3) * 2 : M0 == 1 ? rt & 3 : (rt & 1) * 2;
      if (late) {
        unsigned l2 = late;
#pragma unroll
        for (int d = 64; d >= 8; d >>= 1) {
          const unsigned t = tP - span + (unsigned)d;
          const unsigned l3 = (unsigned)(__builtin_amdgcn_ballot_w64(mk64(hf[0], hf[1]) != tag) | __builtin_amdgcn_ballot_w64((int)(hf[2] - t) > X1)) & 0xFFFFu;
          if (l3) { l2 = l3; break; }
        }
        m = ((31 - __builtin_clz(l2)) >> 2) ^ 3;
      }
      m = __builtin_amdgcn_readfirstlane(m);
      unsigned qso[4];
#pragma unroll
      for (int i = 0; i < 4; ++i)
        asm("s_xor_b32 %0, %1, %3\n\ts_mul_i32 %0, %0, %4\n\ts_add_u32 %0, %0, %2" : "=&s"(qso[i]) : "s"(m), "s"(kvp_grp), "i"(i), "i"(4 * KVP_STR) : "scc");
      auto gate = [&](unsigned mask) {
        if (UNLIKELY(late & mask)) {
#pragma nounroll
          do {
            if (anyl(mk64(hf[0], hf[1]) != tag))
              asm volatile("buffer_load_dwordx4 %0, %1, %2, 0 offen sc1\n\ts_waitcnt vmcnt(0)" : "+v"(hf) : "v"(h1v), "s"(rsS) : "memory");
            late = late_now();
          } while ((late & mask) && ++spins < SPIN_CAP);
        }
      };
      u32x4 g[4][5], zp;
      u64 zc;
#pragma unroll
      for (int i = 0; i < 4; ++i) asm volatile("" : "=v"(g[i][0]), "=v"(g[i][1]), "=v"(g[i][2]), "=v"(g[i][3]), "=v"(g[i][4]));
      asm volatile("" : "=v"(zp), "=v"(zc));
      gate(15u << (4 * m)); quad_load(g[0], gv, rsS, qso[0]);
      gate(15u << (4 * (1 ^ m))); quad_load(g[1], gv, rsS, qso[1]);
      const u64 tag2 = HASHM ? tag + tag : tag;
      wo_a();
      STAMP(19);
      const f32x4 z4 = {0.f, 0.f, 0.f, 0.f};
      f32x4 d[4][2], dz;
      unsigned bad = 0;
      auto sumq = [&](int i) {
        asm volatile("" : "+v"(g[i][0]), "+v"(g[i][1]), "+v"(g[i][2]), "+v"(g[i][3]), "+v"(g[i][4]));
        d[i][0] = mfma16(sel[0], asbf(g[i][0]), z4);
        d[i][1] = mfma16(sel[0], asbf(g[i][2]), z4);
        SB();
        u64 h0 = hash4(g[i][0], tag2, hc), h1 = hash4(g[i][1], tag2, hc);
        PIN("+v"(h0), "+v"(h1));
        SB();
        d[i][0] = mfma16(sel[1], asbf(g[i][1]), d[i][0]);
        d[i][1] = mfma16(sel[1], asbf(g[i][3]), d[i][1]);
        SB();
        h0 = hash4(g[i][2], h0, hc + 4); h1 = hash4(g[i][3], h1, hc + 4);
        if (UNLIKELY(anyl(h0 != mk64(g[i][4][0], g[i][4][1]) || h1 != mk64(g[i][4][2], g[i][4][3])))) { bad |= 1u << i; quad_load(g[i], gv, rsS, qso[i]); }
      };
      auto sumz = [&]() {
        asm volatile("" : "+v"(zp), "+v"(zc));
        dz = mfma16(sel[0], asbf(zp), z4);
        SB();
        if (UNLIKELY(anyl(hash4v(zp, tag, zhc) != zc))) { bad |= 16u; z_load(zp, zc, zv, zcv, rsS, kvp_grp); }
      };
      WAITV(5);
      gate(15u << (4 * (2 ^ m))); quad_load(g[2], gv, rsS, qso[2]);
      sumq(0);
      WAITV(5);
      gate(0xFFFFu); z_load(zp, zc, zv, zcv, rsS, kvp_grp); quad_load(g[3], gv, rsS, qso[3]);
      sumq(1);
      float s0[4], s1[4];
      cnt_wait<4, 4>(cb);
      WAITV(7); sumq(2);
      WAITV(5); sumz();
      WAITV(0); sumq(3);
      if (UNLIKELY(bad)) {
#pragma nounroll
        do {
          WAITV(0);
          const unsigned was = bad; bad = 0;
          if (was & 1) sumq(0);
          if (was & 2) sumq(1);
          if (was & 4) sumq(2);
          if (was & 8) sumq(3);
          if (was & 16) sumz();
        } while (bad && ++spins < SPIN_CAP);
        WAITV(0);
      }
      STAMPV(62, spins);
#pragma unroll
      for (int j = 0; j < 4; ++j) {
        s0[j] = addpp(d[0][0][j], d[1][0][j], d[2][0][j], d[3][0][j]); s1[j] = addpp(d[0][1][j], d[1][1][j], d[2][1][j], d[3][1][j]);
      }
      *(LAS u32x2*)(kw) = u32x2{cvt2(s0[0], s0[1]), cvt2(s0[2], s0[3])};
      *(LAS u32x2*)(kw + 640) = u32x2{cvt2(s1[0], s1[1]), cvt2(s1[2], s1[3])};
      *(LAS u32x2*)(zw) = u32x2{cvt2(dz[0], dz[1]), cvt2(dz[2], dz[3])};
    }
    cnt_arrive<10>(cb, one);
    __builtin_amdgcn_s_setprio(1);
    STAMP(25);
    cnt_wait<10, 4>(cb);
    issue_wo(IC(8 + KB), IC(16));
    cnt_wait<9, 4>(cb);
    issue_wo(IC(16 + Q2), IC(23 + LW1));
    STAMP(26);
    unsigned aor0 = L + L_OWN + lane * 16, aor = L + lane * 16;
    const unsigned epi = L + L_EPI + u * 4096;
    unsigned ew0 = epi + r32 * 64 + (((2 * h) ^ ((r32 >> 1) & 3)) << 4), ew1 = ew0 ^ 16u, er = epi + (lane >> 2) * 64 + (((lane & 3) ^ ((lane >> 3) & 3)) << 4);
    asm volatile("" : "+v"(aor0), "+v"(aor), "+v"(ew0), "+v"(ew1), "+v"(er));
    RAWBAR();
    if (LW1 < 9) { __builtin_amdgcn_s_sleep(3); issue_wo(IC(23 + LW1), IC(32)); }
    f32x16 c2[2] = {zero16(), zero16()};
    bf16x8 ob[64];
#pragma unroll
    for (int hd = 5; hd < 16 + Q2; ++hd) if (hd < 8 + KB || hd >= 16) RW(hd);
    cnt_wait<6, 2>(cb);
#pragma unroll
    for (int i = 0; i < 8; ++i) ob[56 + i] = lds_b128(aor0 + i * 1024);
#pragma unroll
    for (int i = 0; i < 8; ++i) c2[i >> 2] = mfma32(wf[i & 3], ob[56 + i], c2[i >> 2]);
    SB();
    WAITV(0);
    cnt_arrive<5>(cb, one);
    cnt_wait<5, 4>(cb);
#pragma unroll
    for (int hd = 8 + KB; hd < 32; ++hd) if (hd < 16 || hd >= 16 + Q2) RW(hd);
    STAMP(30);
#define RO(n) do { ob[n] = lds_b128(aor + ((n) / 28) * 32768 + ((n) % 28) * 1024); } while (0)
#define MO(n) do { c2[(n) / 28] = mfma32(wf[4 + (n) % 28], ob[n], c2[(n) / 28]); } while (0)
    auto put = [&](int t) {
      *(LAS bf16x8*)(ew0 + t * 2048) = cvt8<0>(c2[t]);
      *(LAS bf16x8*)(ew1 + t * 2048) = cvt8<8>(c2[t]);
      asm volatile("" ::: "memory");
    };
    auto sto = [&](int i, bf16x8 v) { __builtin_amdgcn_raw_buffer_store_b128(asu(v), rsY, ov, i * 32768, 16); };
    BAR();
    STAMP(32);
    SB();
#pragma unroll
    for (int i = 0; i < 8; ++i) RO(i);
    SB();
#pragma unroll
    for (int i = 0; i < 8; ++i) { MO(i); SB(); RO(i + 8); SB(); }
#pragma unroll
    for (int i = 8; i < 12; ++i) MO(i);
    SB();
    RAWBAR();
    SB();
#pragma unroll
    for (int i = 0; i < 4; ++i) { MO(12 + i); SB(); RO(16 + 2 * i); RO(17 + 2 * i); SB(); }
#pragma unroll
    for (int i = 0; i < 4; ++i) { MO(16 + i); SB(); RO(24 + i); SB(); }
#pragma unroll
    for (int i = 20; i < 24; ++i) MO(i);
    SB();
    RAWBAR();
    STAMP(36);
    SB();
#pragma unroll
    for (int i = 0; i < 4; ++i) { MO(24 + i); SB(); RO(28 + 2 * i); RO(29 + 2 * i); SB(); }
#pragma unroll
    for (int i = 0; i < 8; ++i) { MO(28 + i); SB(); RO(36 + i); SB(); }
    bf16x8 e0 = cvt8<0>(c2[0]), e1 = cvt8<8>(c2[0]);
    PIN("+v"(e0), "+v"(e1));
    SB();
    RAWBAR();
    STAMP(38);
    SB();
    MO(36); SB(); *(LAS bf16x8*)(ew0) = e0; SB();
    MO(37); SB(); *(LAS bf16x8*)(ew1) = e1; SB();
    MO(38); SB(); cnt_arrive<8>(cb, one); SB();
    MO(39); SB();
#pragma unroll
    for (int i = 0; i < 4; ++i) { RO(44 + 2 * i); RO(45 + 2 * i); SB(); MO(40 + i); SB(); }
#pragma unroll
    for (int i = 0; i < 4; ++i) { MO(44 + i); SB(); RO(52 + i); SB(); }
#pragma unroll
    for (int i = 48; i < 56; ++i) MO(i);
    SB();
    STAMP(39);
    put(1);
    e0 = lds_b128(er + 2048); e1 = lds_b128(er + 3072);
    sto(2, e0); sto(3, e1);
  }
}

extern "C" void kernel_launch(void* const* d_in, const int* in_sizes, int n_in, void* d_out, int out_size, void* d_ws, size_t ws_size, hipStream_t stream) {
  (void)hipFuncSetAttribute((const void*)hedgehog_fused, hipFuncAttributeMaxDynamicSharedMemorySize, L_BYTES);
  hedgehog_fused<<<256, 512, L_BYTES, stream>>>((const u16*)d_in[0], (const u16*)d_in[1], (const u16*)d_in[4], (const u16*)d_in[5],
                                                (const u16*)d_in[2], (const u16*)d_in[3], (const u16*)d_in[6], (const u16*)d_in[7],
                                                (u16*)d_out, (char*)d_ws);
}
```
